```python
import torch

def _softmax(x, axis=-1):
    return torch.softmax(x, dim=axis)

def _gelu(x):
    return torch.nn.functional.gelu(x, approximate='tanh')

def _silu(x):
    return torch.nn.functional.silu(x)

def _sigmoid(x):
    return torch.sigmoid(x)

def _layernorm(x, axis=-1, eps=1e-05):
    m = x.mean(dim=axis, keepdim=True)
    v = x.var(dim=axis, keepdim=True, unbiased=False)
    return (x - m) / torch.sqrt(v + eps)

def _scatter_add(tgt, idx, src, axis=0):
    return tgt.clone().index_add_(axis, idx, src)

def _segment_sum(src, seg, n):
    out = torch.zeros((n,) + src.shape[1:], dtype=src.dtype)
    return out.index_add_(0, seg, src)

def _pad(x, pw):
    flat = tuple((v for pair in reversed(pw) for v in pair))
    return torch.nn.functional.pad(x, flat)
kernel_name = 'adaLN_cond__sliding_block_cached__geglu_ffn__decode'

def setup_inputs():

    _w = lambda a: (lambda t: t.to(torch.bfloat16) if t.is_floating_point() else t)(torch.as_tensor(a))

    def _A_setup():
        B, Sq, D = (1, 1, 640)
        Hq, Hkv, Sk, Dh = (32, 32, 32768, 192)
        assert Hq % Hkv == 0, (Hq, Hkv)
        return {'x': _w((lambda *s: torch.randn(*s, dtype=torch.bfloat16) * 0.1)(B, Sq, D)), 'k_cache': _w((lambda *s: torch.randn(*s, dtype=torch.bfloat16) * 0.8)(B, Hkv, Sk, Dh)), 'v_cache': _w((lambda *s: torch.randn(*s, dtype=torch.bfloat16) * 1.6)(B, Hkv, Sk, Dh)), 'wq': _w((lambda *s: torch.randn(*s, dtype=torch.bfloat16) * 0.8)(D, Hq * Dh) * D ** (-0.5)), 'wo': _w((lambda *s: torch.randn(*s, dtype=torch.bfloat16) * 1.6)(Hq * Dh, D) * (Hq * Dh) ** (-0.5))}
    _A_in = _A_setup()

    def _B_setup():
        B, S, D, F = (1, 1, 640, 1536)
        return {'x': _w((lambda *s: torch.randn(*s, dtype=torch.bfloat16) * 0.1)(B, S, D)), 'w_gate': _w((lambda *s: torch.randn(*s, dtype=torch.bfloat16) * 0.1)(D, F)), 'w_up': _w((lambda *s: torch.randn(*s, dtype=torch.bfloat16) * 0.1)(D, F)), 'w_down': _w((lambda *s: torch.randn(*s, dtype=torch.bfloat16) * 0.1)(F, D))}
    _B_in = _B_setup()
    return {'A_x': _A_in['x'], 'A_k_cache': _A_in['k_cache'], 'A_v_cache': _A_in['v_cache'], 'A_wq': _A_in['wq'], 'A_wo': _A_in['wo'], 'B_x': _B_in['x'], 'B_w_gate': _B_in['w_gate'], 'B_w_up': _B_in['w_up'], 'B_w_down': _B_in['w_down'], 'c': _w((lambda *s: torch.randn(*s, dtype=torch.bfloat16) * 0.8)(1, 640) * 8.0), 'w_mod': _w((lambda *s: torch.randn(*s, dtype=torch.bfloat16) * 0.8)(640, 6 * 640) * 640 ** (-0.5) * 8.0)}

def reference(A_x, A_k_cache, A_v_cache, A_wq, A_wo, B_x, B_w_gate, B_w_up, B_w_down, c, w_mod):

    def _A_ref(x, k_cache, v_cache, wq, wo):
        B, Sq, D = x.shape
        Hq, Dh = (32, 192)
        Hkv, Sk = (k_cache.shape[1], k_cache.shape[2])
        W = min(4096, max(256, Sk // 4))
        rep = Hq // Hkv
        q = (lambda x, *d: x.permute(*d))((x @ wq).reshape(B, Sq, Hq, Dh), 0, 2, 1, 3)
        k = (lambda x, n, axis: torch.repeat_interleave(x, n, dim=axis))(k_cache[:, :, Sk - W:, :], rep, axis=1)
        v = (lambda x, n, axis: torch.repeat_interleave(x, n, dim=axis))(v_cache[:, :, Sk - W:, :], rep, axis=1)
        scores = torch.einsum('bhqd,bhkd->bhqk', q, k) * Dh ** (-0.5)
        p = _softmax(scores, axis=-1)
        o = torch.einsum('bhqk,bhkd->bhqd', p, v)
        return (lambda x, *d: x.permute(*d))(o, 0, 2, 1, 3).reshape(B, Sq, Hq * Dh) @ wo

    def _B_ref(x, w_gate, w_up, w_down):
        return _gelu(x @ w_gate) * (x @ w_up) @ w_down
    x = A_x
    mod = (_silu(c) @ w_mod).reshape(c.shape[0], 6, 1, -1)
    sa, ca, ga, sb, cb, gb = (mod[:, 0].clone(), mod[:, 1].clone(), mod[:, 2].clone(), mod[:, 3].clone(), mod[:, 4].clone(), mod[:, 5].clone())
    h = _layernorm(x, axis=-1) * (1 + ca) + sa
    x = x + ga * _A_ref(h, A_k_cache, A_v_cache, A_wq, A_wo)
    h = _layernorm(x, axis=-1) * (1 + cb) + sb
    x = x + gb * _B_ref(h, B_w_gate, B_w_up, B_w_down)
    return x
```

```cpp
#include <hip/hip_runtime.h>
#include <hip/hip_bf16.h>
#include <stdint.h>

#define ROUND 0
typedef unsigned short u16;
__device__ __forceinline__ float bf2f(u16 b) { return __builtin_bit_cast(float, (unsigned)b << 16); }
__device__ __forceinline__ u16 f2bf(float f) { __hip_bfloat16 h = __float2bfloat16(f); return __builtin_bit_cast(u16, h); }
__device__ __forceinline__ float rb(float f) { return ROUND ? bf2f(f2bf(f)) : f; }

constexpr int D = 640, HQ = 32, DH = 192, SK = 32768, W = 4096, F = 1536;

__device__ float gemv_col(const float* x, const u16* Wt, int K, int N, int n) {
  float a = 0.f;
  for (int k = 0; k < K; ++k) a = fmaf(x[k], bf2f(Wt[(size_t)k * N + n]), a);
  return a;
}
__device__ float block_sum(float v, float* red) {
  __syncthreads();
  for (int o = 32; o > 0; o >>= 1) v += __shfl_xor(v, o);
  if ((threadIdx.x & 63) == 0) red[threadIdx.x >> 6] = v;
  __syncthreads();
  float s = 0.f;
  for (int i = 0; i < (int)(blockDim.x >> 6); ++i) s += red[i];
  return s;
}
__device__ void ln_mod(const float* xin, const float* shift, const float* scale, float* h, float* red) {
  float s = 0.f;
  for (int i = threadIdx.x; i < D; i += blockDim.x) s += xin[i];
  float mean = block_sum(s, red) / D;
  float q = 0.f;
  for (int i = threadIdx.x; i < D; i += blockDim.x) { float d = xin[i] - mean; q += d * d; }
  float var = block_sum(q, red) / D;
  float m = rb(mean), v = rb(var);
  float sd = rb(sqrtf(rb(v + 1e-5f)));
  for (int i = threadIdx.x; i < D; i += blockDim.x) {
    float ln = rb(rb(xin[i] - m) / sd);
    h[i] = rb(rb(ln * rb(1.f + scale[i])) + shift[i]);
  }
  __syncthreads();
}

__global__ void k_mod(const u16* c, const u16* wmod, float* mod) {
  __shared__ float s[D];
  for (int i = threadIdx.x; i < D; i += blockDim.x) { float v = bf2f(c[i]); s[i] = rb(v / (1.f + expf(-v))); }
  __syncthreads();
  int n = blockIdx.x * blockDim.x + threadIdx.x;
  mod[n] = rb(gemv_col(s, wmod, D, 6 * D, n));
}
__global__ void k_q(const u16* x, const float* mod, const u16* wq, float* q) {
  __shared__ float xs[D], h[D], red[16];
  for (int i = threadIdx.x; i < D; i += blockDim.x) xs[i] = bf2f(x[i]);
  __syncthreads();
  ln_mod(xs, mod, mod + D, h, red);
  int n = blockIdx.x * blockDim.x + threadIdx.x;
  q[n] = rb(gemv_col(h, wq, D, HQ * DH, n));
}
__global__ void k_scores(const float* q, const u16* kc, float* sc) {
  __shared__ float qs[DH];
  int h = blockIdx.y;
  for (int i = threadIdx.x; i < DH; i += blockDim.x) qs[i] = q[h * DH + i];
  __syncthreads();
  int key = blockIdx.x * blockDim.x + threadIdx.x;
  const u16* kr = kc + ((size_t)h * SK + (SK - W) + key) * DH;
  float a = 0.f;
  for (int d = 0; d < DH; ++d) a = fmaf(qs[d], bf2f(kr[d]), a);
  sc[h * W + key] = rb(rb(a) * 0.07216878364870322f);
}
__global__ void k_pv(const float* sc, const u16* vc, float* o) {
  __shared__ float p[W], red[16];
  int h = blockIdx.x;
  float m = -INFINITY;
  for (int i = threadIdx.x; i < W; i += blockDim.x) m = fmaxf(m, sc[h * W + i]);
  for (int o2 = 32; o2 > 0; o2 >>= 1) m = fmaxf(m, __shfl_xor(m, o2));
  if ((threadIdx.x & 63) == 0) red[threadIdx.x >> 6] = m;
  __syncthreads();
  for (int i = 0; i < (int)(blockDim.x >> 6); ++i) m = fmaxf(m, red[i]);
  float s = 0.f;
  for (int i = threadIdx.x; i < W; i += blockDim.x) { float e = expf(sc[h * W + i] - m); p[i] = e; s += e; }
  float L = block_sum(s, red);
  for (int i = threadIdx.x; i < W; i += blockDim.x) p[i] = rb(p[i] / L);
  __syncthreads();
  if (threadIdx.x < DH) {
    const u16* vb = vc + ((size_t)h * SK + (SK - W)) * DH;
    o[h * DH + threadIdx.x] = rb(gemv_col(p, vb, W, DH, threadIdx.x));
  }
}
__global__ void k_wo(const float* o, const u16* wo, const u16* x, const float* mod, float* x1) {
  __shared__ float os[HQ * DH];
  for (int i = threadIdx.x; i < HQ * DH; i += blockDim.x) os[i] = o[i];
  __syncthreads();
  int n = blockIdx.x * blockDim.x + threadIdx.x;
  float a = rb(gemv_col(os, wo, HQ * DH, D, n));
  x1[n] = rb(bf2f(x[n]) + rb(mod[2 * D + n] * a));
}
__global__ void k_gu(const float* x1, const float* mod, const u16* wg, const u16* wu, float* g) {
  __shared__ float xs[D], h[D], red[16];
  for (int i = threadIdx.x; i < D; i += blockDim.x) xs[i] = x1[i];
  __syncthreads();
  ln_mod(xs, mod + 3 * D, mod + 4 * D, h, red);
  int n = blockIdx.x * blockDim.x + threadIdx.x;
  float a = rb(gemv_col(h, wg, D, F, n)), b = rb(gemv_col(h, wu, D, F, n));
  float ge = rb(0.5f * a * (1.f + tanhf(0.7978845608028654f * (a + 0.044715f * a * a * a))));
  g[n] = rb(ge * b);
}
__global__ void k_down(const float* g, const u16* wd, const float* x1, const float* mod, u16* out) {
  __shared__ float gs[F];
  for (int i = threadIdx.x; i < F; i += blockDim.x) gs[i] = g[i];
  __syncthreads();
  int n = blockIdx.x * blockDim.x + threadIdx.x;
  float a = rb(gemv_col(gs, wd, F, D, n));
  out[n] = f2bf(x1[n] + rb(mod[5 * D + n] * a));
}

extern "C" void kernel_launch(void* const* d_in, const int* in_sizes, int n_in,
                              void* d_out, int out_size, void* d_ws, size_t ws_size,
                              hipStream_t stream) {
  const u16* x = (const u16*)d_in[0]; const u16* kc = (const u16*)d_in[1]; const u16* vc = (const u16*)d_in[2];
  const u16* wq = (const u16*)d_in[3]; const u16* wo = (const u16*)d_in[4];
  const u16* wg = (const u16*)d_in[6]; const u16* wu = (const u16*)d_in[7]; const u16* wd = (const u16*)d_in[8];
  const u16* c = (const u16*)d_in[9]; const u16* wmod = (const u16*)d_in[10];
  float* ws = (float*)d_ws;
  float* mod = ws; float* q = ws + 4096; float* sc = ws + 16384; float* o = sc + HQ * W; float* x1 = o + 8192; float* g = x1 + 1024;
  k_mod<<<60, 64, 0, stream>>>(c, wmod, mod);
  k_q<<<96, 64, 0, stream>>>(x, mod, wq, q);
  k_scores<<<dim3(W / 64, HQ), 64, 0, stream>>>(q, kc, sc);
  k_pv<<<HQ, 256, 0, stream>>>(sc, vc, o);
  k_wo<<<10, 64, 0, stream>>>(o, wo, x, mod, x1);
  k_gu<<<24, 64, 0, stream>>>(x1, mod, wg, wu, g);
  k_down<<<10, 64, 0, stream>>>(g, wd, x1, mod, (u16*)d_out);
}
```

```cpp
#include <hip/hip_runtime.h>
#include <stdint.h>

typedef unsigned u32;
typedef unsigned long long u64;
typedef u32 v4u __attribute__((ext_vector_type(4)));
typedef u32 v3u __attribute__((ext_vector_type(3)));
typedef u32 v2u __attribute__((ext_vector_type(2)));
typedef float v4f __attribute__((ext_vector_type(4)));
typedef float v2f __attribute__((ext_vector_type(2)));
typedef __bf16 bf16x2 __attribute__((ext_vector_type(2)));
typedef __amdgpu_buffer_rsrc_t rsrc_t;
#define DEV __device__ __forceinline__
#define UNLIKELY(x) (!(ABL & 1) && __builtin_expect(!!(x), 0))
#define LAS __attribute__((address_space(3)))

#define PWK 3
#define PWKD 5
#define PWV 4
#define PWVD 6
#define NAHEAD 24
#define KFIRST 2
#define HPOS 9
#define QPOS 22
#define VQK 4
#define STPOS 18
#define CODEPF 1
#define ABL 0

constexpr int D = 640, DH = 192, SK = 32768, WIN = 4096;
constexpr int NT = 2, SC1 = 16;
constexpr u32 SENT = 0xFFFFFFFFu;
constexpr u32 SPIN_CAP = (ABL & 1) ? 1 : 1u << 16;
constexpr int WS_E = 0, WS_MOD = 1024, WS_Q = 16384, WS_ST = 40960, WS_OA = 45056, WS_AB = WS_OA + 98304, WS_FC = WS_AB + 20480,
              WS_END = WS_FC + 61440;
constexpr int L_CNT = 0, L_ST = 64, L_Q = 128, L_RED = 512, L_R2 = L_RED + 8 * 768, L_G = L_R2 + 4096, L_END = L_G + 1024;

struct Params {
  const void* kc; const void* vc; void* ws; const void* wq; const void* wo; const void* wg; const void* wmod; const void* c;
  const void* x; const void* wu; const void* wd; void* out;
};

DEV rsrc_t mk(const void* p, u32 bytes) { return __builtin_amdgcn_make_buffer_rsrc((void*)p, 0, bytes, 0x00020000); }
template <int AUX> DEV v4u ld4(rsrc_t r, int v, int s) { return __builtin_amdgcn_raw_buffer_load_b128(r, v, s, AUX); }
template <int AUX> DEV v3u ld3(rsrc_t r, int v, int s) { return __builtin_amdgcn_raw_buffer_load_b96(r, v, s, AUX); }
template <int AUX> DEV v2u ld2(rsrc_t r, int v, int s) { return __builtin_amdgcn_raw_buffer_load_b64(r, v, s, AUX); }
template <int AUX> DEV u32 ld1(rsrc_t r, int v, int s) { return __builtin_amdgcn_raw_buffer_load_b32(r, v, s, AUX); }
DEV void st4(rsrc_t r, int v, int s, v4u d) { __builtin_amdgcn_raw_buffer_store_b128(d, r, v, s, SC1); }
DEV float f_(u32 x) { return __builtin_bit_cast(float, x); }
DEV u32 u_(float x) { return __builtin_bit_cast(u32, x); }
DEV float lo_(u32 x) { return f_(x << 16); }
DEV float hi_(u32 x) { return f_(x & 0xffff0000u); }
DEV u32 pk(float a, float b) { bf16x2 t = {(__bf16)a, (__bf16)b}; return __builtin_bit_cast(u32, t); }
DEV float rb(float a) { return (float)(__bf16)a; }
DEV float rcp(float a) { return __builtin_amdgcn_rcpf(a); }
DEV u32 nz(u32 x) { return x < 0xFFFFFFFEu ? x : 0xFFFFFFFEu; }
DEV u32 umax(u32 a, u32 b) { return a > b ? a : b; }
DEV u32 top4(v4u x) { return umax(umax(x.x, x.y), umax(x.z, x.w)); }
DEV bool any(bool c) { return __builtin_amdgcn_ballot_w64(c) != 0; }
DEV float dot2(u32 a, u32 b, float c) {
  return __builtin_amdgcn_fdot2_f32_bf16(__builtin_bit_cast(bf16x2, a), __builtin_bit_cast(bf16x2, b), c, false);
}
template <int CTRL> DEV float dpp(float x) { return f_(__builtin_amdgcn_mov_dpp(u_(x), CTRL, 0xf, 0xf, true)); }
template <int CTRL> DEV u32 dppu(u32 x) { return __builtin_amdgcn_mov_dpp(x, CTRL, 0xf, 0xf, true); }
constexpr int X1 = 0xB1, X2 = 0x4E, X3 = 0x1B, HM = 0x141, R8 = 0x128;
template <int AND, int OR> DEV u32 swz(u32 x) { return __builtin_amdgcn_ds_swizzle(x, AND | (OR << 5)); }
DEV float sw32(float a, float b) {
  auto t = __builtin_amdgcn_permlane32_swap(u_(a), u_(b), false, false); return f_(t[0]) + f_(t[1]);
}
DEV float sw16(float a, float b) {
  auto t = __builtin_amdgcn_permlane16_swap(u_(a), u_(b), false, false); return f_(t[0]) + f_(t[1]);
}
DEV float mx32(float a) { auto t = __builtin_amdgcn_permlane32_swap(u_(a), u_(a), false, false); return fmaxf(f_(t[0]), f_(t[1])); }
DEV float mx16(float a) { auto t = __builtin_amdgcn_permlane16_swap(u_(a), u_(a), false, false); return fmaxf(f_(t[0]), f_(t[1])); }
DEV float sum8(float a) { a += dpp<X1>(a); a += dpp<X2>(a); a += dpp<HM>(a); return a; }
DEV float sumhi(float a) { a += dpp<R8>(a); a = sw16(a, a); return sw32(a, a); }
DEV float max8(float a) { a = fmaxf(a, dpp<X1>(a)); a = fmaxf(a, dpp<X2>(a)); return fmaxf(a, dpp<HM>(a)); }
DEV float maxhi(float a) { a = fmaxf(a, dpp<R8>(a)); return mx32(mx16(a)); }
DEV float sum64(float a) { return sumhi(sum8(a)); }
DEV float ex(float a) { return __builtin_amdgcn_exp2f(a * 1.4426950408889634f); }
DEV u32 plo(u32 a, u32 b) { return __builtin_amdgcn_perm(b, a, 0x05040100u); }
DEV u32 phi(u32 a, u32 b) { return __builtin_amdgcn_perm(b, a, 0x07060302u); }
#define SB() __builtin_amdgcn_sched_barrier(0)
#define PACE2(deep, n) do { asm volatile("s_waitcnt vmcnt(%1)\n\ts_cmp_lt_u32 %0, %3\n\ts_cbranch_scc1 1f\n\ts_waitcnt vmcnt(%2)\n1:" \
                              :: "s"(ahead), "n"((deep) - 1), "n"((n) - 1), "n"(NAHEAD) : "memory", "scc"); SB(); } while (0)
#define LWAIT() asm volatile("s_waitcnt lgkmcnt(0)" ::: "memory")
#define BAR() do { asm volatile("s_waitcnt lgkmcnt(0)\n\ts_barrier" ::: "memory"); SB(); } while (0)
#define CFENCE() asm volatile("" ::: "memory")
#define PIN(x) asm volatile("" : "+v"(x))
constexpr int OOB = 0x7ffffff0;

#define STAMP(i)

DEV void pair_rows(const v4u a, const v4u b, v4u& p0, v4u& p1) {
  p0 = v4u{plo(a.x, b.x), phi(a.x, b.x), plo(a.y, b.y), phi(a.y, b.y)};
  p1 = v4u{plo(a.z, b.z), phi(a.z, b.z), plo(a.w, b.w), phi(a.w, b.w)};
  PIN(p0); PIN(p1);
}
template <int J> DEV u32 bc8(u32 a) { return swz<0x18, J>(a); }
DEV void spread(u32 a, u32 (&s)[6]) { s[0] = bc8<0>(a); s[1] = bc8<1>(a); s[2] = bc8<2>(a); s[3] = bc8<3>(a); s[4] = bc8<4>(a); s[5] = bc8<5>(a); }
template <int N> DEV float tile_dot(const v4u (&W)[12], const u32 (&s)[6], int l) {
  float a[8];
#pragma unroll
  for (int c = 0; c < 8; ++c) a[c] = 0.f;
#pragma unroll
  for (int j = 0; j < N; ++j)
#pragma unroll
    for (int c = 0; c < 8; ++c) a[c] = dot2(W[2 * j + (c >> 2)][c & 3], s[j], a[c]);
  const float y0 = sw32(a[0], a[4]), y1 = sw32(a[1], a[5]), y2 = sw32(a[2], a[6]), y3 = sw32(a[3], a[7]);
  float z0 = sw16(y0, y2), z1 = sw16(y1, y3);
  z0 += dpp<R8>(z0); z1 += dpp<R8>(z1);
  return (l & 8) ? z1 : z0;
}
DEV int tile_col(int l) { return (l & 7) * 8 + (l >> 5) * 4 + ((l >> 4) & 1) * 2 + ((l >> 3) & 1); }
DEV float col_sum(const char* p) {
  const float* f = (const float*)p;
  return ((f[0] + f[64]) + (f[128] + f[192])) + ((f[256] + f[320]) + (f[384] + f[448]));
}
DEV bool last_of_eight(char* lds, int which) {
  LWAIT();
  u32 old = 0;
  if ((threadIdx.x & 63) == 0) old = __hip_atomic_fetch_add((LAS u32*)(lds + L_CNT) + which, 1u, __ATOMIC_RELAXED, __HIP_MEMORY_SCOPE_WORKGROUP);
  const bool last = __builtin_amdgcn_readfirstlane(old) == 7;
  CFENCE();
  return last;
}
DEV float silu_b(float v) { return v * rcp(1.f + ex(-v)); }

__global__ void __launch_bounds__(512, 2) fused_block(Params P) {
  __shared__ __attribute__((aligned(16))) char lds[L_END];
  STAMP(0);
  const int b = blockIdx.x, l = threadIdx.x & 63, w = __builtin_amdgcn_readfirstlane(threadIdx.x >> 6);
  const int xg = b & 7, idx = b >> 3, head = 4 * xg + (idx >> 3), sp = idx & 7;
  const int r8 = l >> 3, c8 = l & 7;
  const size_t kvbase = ((size_t)head * SK + (SK - WIN) + sp * 512 + w * 64) * (DH * 2);
  const rsrc_t rk = mk((const char*)P.kc + kvbase, 64 * 384);
  const int kvo = r8 * 384 + c8 * 16;
  v4u K[24];
#pragma unroll
  for (int i = 0; i < KFIRST; ++i) K[i] = ld4<NT>(rk, kvo + (i % 3) * 128, (i / 3) * 3072);
  SB();
  u32 cpf = 0;
  {
    u64 pc; u32 len;
    asm volatile("s_getpc_b64 %0\n.Lcode_begin:\n\ts_mov_b32 %1, .Lcode_end-.Lcode_begin" : "=s"(pc), "=s"(len));
    cpf = ld1<0>(mk((const void*)pc, len), l == 0 ? (idx * 8 + w) * 128 : OOB, 0);
    SB();
  }
  const bool isQ = idx < 12, isO = idx >= 12 && idx < 22, isG = idx >= 22 && idx < 25, isM = idx >= 25;
  const int mu = (idx - 25) * 8 + xg;
  const int unit = isQ ? 12 * xg + idx : isO ? idx - 12 : isG ? 3 * xg + idx - 22 : mu;
  const int stride = isQ ? 12288 : isO ? 1280 : isG ? 3072 : 7680;
  const int nrow = isO ? 12 : 10;
  const int row0 = (isO ? 768 * xg : 0) + w * 8 * nrow;
  const char* wbase = (const char*)(isQ ? P.wq : isO ? P.wo : isG ? P.wg : P.wmod);
  const int woff = row0 * stride + unit * 128, wlen = 8 * nrow * stride - unit * 128;
  const rsrc_t rw = mk(wbase + woff, (ABL & 4) ? 0 : wlen);
  const int wvo = r8 * nrow * stride + c8 * 16;
  const rsrc_t rws = mk(P.ws, (ABL & 2) ? 0 : WS_END);
  const rsrc_t rz = mk(P.ws, 0);
#define RW(bit) ((ABL & (bit)) ? rz : rws)
  const u32 ahead = b - 176;
  const u64 ecall = *(const __attribute__((address_space(4))) u64*)P.ws;
  const u64 tag64 = ecall * 6364136223846793005ull + 1442695040888963407ull;
  const u32 tag = (u32)tag64;

  if (isM && mu < 20) {
    const v4u e = {SENT, SENT, SENT, SENT};
    st4(RW(8), l * 16, WS_OA + (mu * 8 + w) * 1024, e);
    st4(RW(8), l * 16, WS_OA + (160 + mu * 8 + w) * 1024, e);
  }
  const int pe = w * 80 + r8 * 10 + (c8 < 5 ? c8 : 4) * 2;
  const u32 ap = ld1<0>(mk(isM ? P.c : P.x, 1280), pe * 2, 0);
  v4u xa; u32 xb;
  if (isQ) { xa = ld4<0>(mk(P.x, 1280), l * 20, 0); xb = ld1<0>(mk(P.x, 1280), l * 20 + 16, 0); }
  if (threadIdx.x == 0) *(v2u*)(lds + L_CNT) = v2u{0u, 0u};
  SB();
  v4u W[12];
#pragma unroll
  for (int i = 0; i < 10; ++i) {
    W[i] = ld4<NT>(rw, wvo, i * stride);
    SB();
    if (i == 0) BAR();
    if (KFIRST + i >= PWK - 1) PACE2(PWKD, PWK);
  }
  auto kload = [&](int i) { K[i] = ld4<NT>(rk, kvo + (i % 3) * 128, (i / 3) * 3072); SB(); PACE2(PWKD, PWK); };
  constexpr int MPOS = KFIRST + PWK;
#pragma unroll
  for (int i = KFIRST; i < MPOS; ++i) kload(i);
#pragma unroll
  for (int j = 0; j < 5; ++j) pair_rows(W[2 * j], W[2 * j + 1], W[2 * j], W[2 * j + 1]);
  SB();
  STAMP(1);
  auto publish_tagged = [&](int base) {
    const float v = col_sum(lds + L_R2 + l * 4);
    const u32 p0 = pk(v, dpp<X1>(v)), p1 = dppu<X2>(p0);
    st4(RW(64), (l & 3) == 0 ? l * 4 : OOB, base, v4u{p0, tag, p1, tag});
  };
  float ln0 = 0.f, ln1 = 0.f;
  if (isM && mu < 50) {
    u32 s[6];
    spread(pk(silu_b(lo_(ap)), silu_b(hi_(ap))), s);
    *(float*)(lds + L_R2 + w * 256 + tile_col(l) * 4) = tile_dot<5>(W, s, l);
    if (last_of_eight(lds, 0)) publish_tagged(WS_MOD + mu * 256);
    SB();
    STAMP(2);
  } else if (isQ) {
    float v[10] = {lo_(xa.x), hi_(xa.x), lo_(xa.y), hi_(xa.y), lo_(xa.z), hi_(xa.z), lo_(xa.w), hi_(xa.w), lo_(xb), hi_(xb)};
    float s = 0.f, q = 0.f;
#pragma unroll
    for (int i = 0; i < 10; ++i) s += v[i];
    const float mean = sum64(s) * (1.f / D);
#pragma unroll
    for (int i = 0; i < 10; ++i) { const float d = v[i] - mean; q = fmaf(d, d, q); }
    const float var = sum64(q) * (1.f / D);
    const float m = rb(mean), rs = rcp(rb(__builtin_amdgcn_sqrtf(rb(rb(var) + 1e-5f))));
    ln0 = rb(rb(lo_(ap) - m) * rs); ln1 = rb(rb(hi_(ap) - m) * rs);
    PIN(ln0); PIN(ln1);
    SB();
  }
#pragma unroll
  for (int i = MPOS; i < HPOS; ++i) kload(i);
  v2u gs, gc;
  if (isQ) { gs = ld2<SC1>(RW(128), pe * 4, WS_MOD); gc = ld2<SC1>(RW(128), pe * 4, WS_MOD + 2560); SB(); }
#pragma unroll
  for (int i = HPOS; i < HPOS + PWK; ++i) kload(i);
  if (isQ) {
    if (UNLIKELY(any((gs.y != tag) | (gc.y != tag)))) {
      for (u32 spin = 0; spin < SPIN_CAP; ++spin) {
        CFENCE();
        gs = ld2<SC1>(rws, pe * 4, WS_MOD); gc = ld2<SC1>(rws, pe * 4, WS_MOD + 2560);
        if (!any((gs.y != tag) | (gc.y != tag))) break;
      }
    }
    STAMP(2);
    u32 s[6];
    spread(pk(rb(ln0 * rb(1.f + lo_(gc.x))) + lo_(gs.x), rb(ln1 * rb(1.f + hi_(gc.x))) + hi_(gs.x)), s);
    *(float*)(lds + L_R2 + w * 256 + tile_col(l) * 4) = tile_dot<5>(W, s, l);
    if (last_of_eight(lds, 0)) publish_tagged(WS_Q + unit * 256);
    SB();
    STAMP(3);
  }
#pragma unroll
  for (int i = HPOS + PWK; i < QPOS; ++i) kload(i);
  const int qo = (l < 48 ? l : 0) * 16;
  v4u qg = ld4<SC1>(RW(32), qo, WS_Q + head * 768);
  SB();
#pragma unroll
  for (int i = QPOS; i < 24; ++i) kload(i);
  const rsrc_t rv = mk((const char*)P.vc + kvbase, 64 * 384);
  const int vvo = (l >> 5) * 4 * 384 + (l & 31) * 12;
  v3u V[32];
  auto vload = [&](int i) { V[i] = ld3<NT>(rv, vvo + (i & 3) * 384, (i >> 2) * 3072); SB(); PACE2(PWVD, PWV); };
#pragma unroll
  for (int i = 0; i < VQK; ++i) vload(i);
  asm volatile("" :: "v"(cpf));
  STAMP(4);
  if (UNLIKELY(any((qg.y != tag) | (qg.w != tag)))) {
    for (u32 spin = 0; spin < SPIN_CAP; ++spin) {
      CFENCE();
      qg = ld4<SC1>(rws, qo, WS_Q + head * 768);
      if (!any((qg.y != tag) | (qg.w != tag))) break;
    }
  }
  STAMP(5);
  float p[8], mw;
  {
    *(v2u*)(lds + L_Q + (qo >> 1)) = v2u{qg.x, qg.z};
    LWAIT();
    v4u qf[3];
#pragma unroll
    for (int k = 0; k < 3; ++k) qf[k] = *(const v4u*)(lds + L_Q + k * 128 + c8 * 16);
    float m = -INFINITY;
#pragma unroll
    for (int t = 0; t < 8; ++t) {
      float s = 0.f;
#pragma unroll
      for (int k = 0; k < 3; ++k)
#pragma unroll
        for (int d = 0; d < 4; ++d) s = dot2(K[3 * t + k][d], qf[k][d], s);
      s = rb(rb(sum8(s)) * 0.07216878364870322f);
      p[t] = s; m = fmaxf(m, s);
      if (t == 3) { PIN(p[0]); PIN(p[1]); PIN(p[2]); PIN(p[3]); SB(); vload(VQK); }
    }
    mw = maxhi(m);
    float ls = 0.f;
#pragma unroll
    for (int t = 0; t < 8; ++t) { p[t] = ex(p[t] - mw); ls += p[t]; }
    ls = sumhi(ls);
    *(v2f*)(lds + L_ST + w * 8) = v2f{mw, ls};
    SB();
    vload(VQK + 1);
    if (last_of_eight(lds, 1)) {
      const v2f t = *(const v2f*)(lds + L_ST + c8 * 8);
      const float mc = max8(t[0]), lc = sum8(t[1] * ex(t[0] - mc));
      st4(RW(16), l == 0 ? 0 : OOB, WS_ST + (head * 8 + sp) * 16, v4u{u_(mc), tag, u_(lc), tag});
    }
    SB();
  }
  STAMP(14);
  v2f acc[3] = {{0.f, 0.f}, {0.f, 0.f}, {0.f, 0.f}};
  float pb[32];
  auto pspread = [&](int t) {
    pb[4 * t] = f_(swz<0, 0>(u_(p[t]))); pb[4 * t + 1] = f_(swz<0, 8>(u_(p[t])));
    pb[4 * t + 2] = f_(swz<0, 16>(u_(p[t]))); pb[4 * t + 3] = f_(swz<0, 24>(u_(p[t])));
    if (t == 7) { PIN(pb[28]); PIN(pb[29]); PIN(pb[30]); PIN(pb[31]); }
  };
  auto pv = [&](int i) {
    if ((i & 3) == 0 && i < 28) pspread(i >> 2);
    const v2f pp = {pb[i], pb[i]};
#pragma unroll
    for (int d = 0; d < 3; ++d) acc[d] = __builtin_elementwise_fma(pp, v2f{lo_(V[i][d]), hi_(V[i][d])}, acc[d]);
    asm volatile("" : "+v"(acc[0]), "+v"(acc[1]), "+v"(acc[2]));
    SB();
  };
  v4u st;
  constexpr int STCHK = 28;
  static_assert(STPOS + PWVD <= 31, "statistics must have landed");
  constexpr int LAG = PWVD - 1, FIRST = VQK + 2;
#pragma unroll
  for (int t = 0; t < FIRST - LAG; ++t) pv(t);
#pragma unroll
  for (int i = FIRST; i < 32; ++i) {
    if (i == STPOS) { st = ld4<SC1>(RW(256), c8 * 16, WS_ST + head * 128); SB(); }
    vload(i);
    if (i - LAG >= 0 && i - LAG < STCHK) pv(i - LAG);
  }
  constexpr int done = 32 - LAG < STCHK ? 32 - LAG : STCHK;
  {
    const rsrc_t r2 = mk(wbase + woff, isO ? wlen : 0);
    W[10] = ld4<NT>(r2, wvo, 10 * stride); W[11] = ld4<NT>(r2, wvo, 11 * stride);
    SB();
  }
  STAMP(6);
  if (UNLIKELY(any((st.y != tag) | (st.w != tag)))) {
    for (u32 spin = 0; spin < SPIN_CAP; ++spin) {
      CFENCE();
      st = ld4<SC1>(rws, c8 * 16, WS_ST + head * 128);
      if (!any((st.y != tag) | (st.w != tag))) break;
    }
  }
  const float M = max8(f_(st.x));
  const float scale = ex(mw - M) * rcp(sum8(f_(st.z) * ex(f_(st.x) - M)));
  p[7] *= scale;
  pspread(7);
  SB();
#pragma unroll
  for (int t = done; t < STCHK; ++t) pv(t);
#pragma unroll
  for (int d = 0; d < 3; ++d) acc[d] *= scale;
#pragma unroll
  for (int t = STCHK; t < 32; ++t) pv(t);
  STAMP(7);
  {
    float* dst = (float*)(lds + L_RED + w * 768 + (l & 31) * 24 + (l >> 5) * 12);
    dst[0] = sw32(acc[0][0], acc[1][1]); dst[1] = sw32(acc[0][1], acc[2][0]); dst[2] = sw32(acc[1][0], acc[2][1]);
  }
  BAR();
  if (w == 7) {
    const char* src = lds + L_RED + qo;
    v4f a = *(const v4f*)src;
#pragma unroll
    for (int i = 1; i < 8; ++i) a += *(const v4f*)(src + i * 768);
    const u32 d0 = nz(pk(a[0], a[1])), d1 = nz(pk(a[2], a[3]));
    const u32 e0 = dppu<X1>(d0), e1 = dppu<X1>(d1);
    st4(rws, (l < 48 && !(l & 1)) ? l * 8 : OOB, WS_OA + (head * 8 + sp) * 384, v4u{d0, d1, e0, e1});
  }
  SB();
  STAMP(8);

  if (isO) {
    pair_rows(W[10], W[11], W[10], W[11]);
    u32 one_lo = 0x00003f80u, one_hi = 0x3f800000u;
    PIN(one_lo); PIN(one_hi);
    SB();
    STAMP(9);
    u32 a[8];
    const int ao = (r8 * 6 + (c8 < 6 ? c8 : 5)) * 4, as = WS_OA + (4 * xg + (w >> 1)) * 3072 + (w & 1) * 192;
    for (u32 spin = 0; spin < SPIN_CAP; ++spin) {
      CFENCE();
#pragma unroll
      for (int s = 0; s < 8; ++s) a[s] = ld1<SC1>(rws, ao + s * 384, as);
      if (!any(umax(umax(umax(a[0], a[1]), umax(a[2], a[3])), umax(umax(a[4], a[5]), umax(a[6], a[7]))) == SENT)) break;
    }
    STAMP(10);
    float o0 = 0.f, o1 = 0.f, o2 = 0.f, o3 = 0.f;
#pragma unroll
    for (int s = 0; s < 4; ++s) {
      o0 = dot2(a[s], one_lo, o0); o1 = dot2(a[s], one_hi, o1);
      o2 = dot2(a[4 + s], one_lo, o2); o3 = dot2(a[4 + s], one_hi, o3);
    }
    u32 s[6];
    spread(pk(o0 + o2, o1 + o3), s);
    *(float*)(lds + L_R2 + w * 256 + tile_col(l) * 4) = tile_dot<6>(W, s, l);
    BAR();
    if (w == 0) {
      const u32 v = nz(u_(col_sum(lds + L_R2 + l * 4)));
      st4(rws, (l & 3) == 0 ? l * 4 : OOB, WS_AB + xg * 2560 + unit * 256, v4u{v, dppu<X1>(v), dppu<X2>(v), dppu<X3>(v)});
    }
    STAMP(11);
  } else if (isG) {
    BAR();
    v4u U[12];
    v2u Dn[32];
    const rsrc_t ru = mk((const char*)P.wu + woff, wlen);
#pragma unroll
    for (int i = 0; i < 10; ++i) U[i] = ld4<NT>(ru, wvo, i * 3072);
    v2u gg, gb2, gc2;
    gg = ld2<SC1>(rws, pe * 4, WS_MOD + 2 * 2560); gb2 = ld2<SC1>(rws, pe * 4, WS_MOD + 3 * 2560); gc2 = ld2<SC1>(rws, pe * 4, WS_MOD + 4 * 2560);
    const rsrc_t rd = mk((const char*)P.wd + unit * 64 * 1280, w < 5 ? 64 * 1280 : 0);
#pragma unroll
    for (int i = 0; i < 32; ++i) Dn[i] = ld2<NT>(rd, (l >> 5) * 32 * 1280 + (l & 31) * 8, i * 1280 + w * 256);
    SB();
    if (UNLIKELY(any((gg.y != tag) | (gb2.y != tag) | (gc2.y != tag)))) {
      for (u32 spin = 0; spin < SPIN_CAP; ++spin) {
        CFENCE();
        gg = ld2<SC1>(rws, pe * 4, WS_MOD + 2 * 2560); gb2 = ld2<SC1>(rws, pe * 4, WS_MOD + 3 * 2560); gc2 = ld2<SC1>(rws, pe * 4, WS_MOD + 4 * 2560);
        if (!any((gg.y != tag) | (gb2.y != tag) | (gc2.y != tag))) break;
      }
    }
#pragma unroll
    for (int j = 0; j < 5; ++j) pair_rows(U[2 * j], U[2 * j + 1], U[2 * j], U[2 * j + 1]);
    v4u Dp[16];
#pragma unroll
    for (int j = 0; j < 16; ++j) {
      Dp[j] = v4u{plo(Dn[2 * j].x, Dn[2 * j + 1].x), phi(Dn[2 * j].x, Dn[2 * j + 1].x), plo(Dn[2 * j].y, Dn[2 * j + 1].y), phi(Dn[2 * j].y, Dn[2 * j + 1].y)};
      PIN(Dp[j]);
    }
    float ga0 = lo_(gg.x), ga1 = hi_(gg.x), sb0 = lo_(gb2.x), sb1 = hi_(gb2.x), cb0 = rb(1.f + lo_(gc2.x)), cb1 = rb(1.f + hi_(gc2.x));
    float xv0 = lo_(ap), xv1 = hi_(ap), live = c8 < 5 ? 1.f : 0.f;
    PIN(ga0); PIN(ga1); PIN(sb0); PIN(sb1); PIN(cb0); PIN(cb1); PIN(xv0); PIN(xv1); PIN(live);
    SB();
    STAMP(9);
    v2u a[8];
    for (u32 spin = 0; spin < SPIN_CAP; ++spin) {
      CFENCE();
      u32 top = 0;
#pragma unroll
      for (int g = 0; g < 8; ++g) a[g] = ld2<SC1>(rws, pe * 4, WS_AB + g * 2560);
#pragma unroll
      for (int g = 0; g < 8; ++g) top = umax(top, umax(a[g].x, a[g].y));
      if (!any(top == SENT)) break;
    }
    STAMP(10);
    const v2f sm = ((__builtin_bit_cast(v2f, a[0]) + __builtin_bit_cast(v2f, a[1])) + (__builtin_bit_cast(v2f, a[2]) + __builtin_bit_cast(v2f, a[3]))) +
                   ((__builtin_bit_cast(v2f, a[4]) + __builtin_bit_cast(v2f, a[5])) + (__builtin_bit_cast(v2f, a[6]) + __builtin_bit_cast(v2f, a[7])));
    const float x0 = rb(xv0 + rb(ga0 * rb(sm[0]))), x1 = rb(xv1 + rb(ga1 * rb(sm[1])));
    float t = sw32((x0 + x1) * live, fmaf(x0, x0, x1 * x1) * live);
    t = sum8(t); t += dpp<R8>(t); t = sw16(t, t);
    *(float*)(lds + L_ST + (l >> 5) * 32 + w * 4) = t;
    BAR();
    const v4f s0 = *(const v4f*)(lds + L_ST), s1 = *(const v4f*)(lds + L_ST + 16), q0 = *(const v4f*)(lds + L_ST + 32), q1 = *(const v4f*)(lds + L_ST + 48);
    const v4f ss = s0 + s1, qq = q0 + q1;
    const float mean = ((ss[0] + ss[1]) + (ss[2] + ss[3])) * (1.f / D);
    const float var = fmaxf(fmaf(-mean, mean, ((qq[0] + qq[1]) + (qq[2] + qq[3])) * (1.f / D)), 0.f);
    const float m = rb(mean), rs = rcp(rb(__builtin_amdgcn_sqrtf(rb(rb(var) + 1e-5f))));
    u32 s[6];
    spread(pk(rb(rb(rb(x0 - m) * rs) * cb0) + sb0, rb(rb(rb(x1 - m) * rs) * cb1) + sb1), s);
    const float tg = tile_dot<5>(W, s, l), tu = tile_dot<5>(U, s, l);
    *(float*)(lds + L_R2 + w * 256 + tile_col(l) * 4) = tg;
    *(float*)(lds + L_R2 + 2048 + w * 256 + tile_col(l) * 4) = tu;
    BAR();
    if (w < 5) {
      const float gt = rb(col_sum(lds + L_R2 + l * 4)), up = rb(col_sum(lds + L_R2 + 2048 + l * 4));
      const float u = 0.7978845608028654f * (gt + 0.044715f * gt * gt * gt);
      const float th = 1.f - 2.f * rcp(1.f + ex(2.f * u));
      const float g = rb(0.5f * gt * (1.f + th)) * up;
      *(__bf16*)(lds + L_G + w * 128 + l * 2) = (__bf16)g;
      LWAIT();
      v4u gp[4];
#pragma unroll
      for (int i = 0; i < 4; ++i) gp[i] = *(const v4u*)(lds + L_G + w * 128 + (l >> 5) * 64 + i * 16);
      float y[4] = {0.f, 0.f, 0.f, 0.f};
#pragma unroll
      for (int j = 0; j < 16; ++j)
#pragma unroll
        for (int c = 0; c < 4; ++c) y[c] = dot2(Dp[j][c], gp[j >> 2][j & 3], y[c]);
      v4u o;
#pragma unroll
      for (int c = 0; c < 4; ++c) o[c] = nz(u_(sw32(y[c], y[c])));
      st4(rws, l < 32 ? l * 16 : OOB, WS_FC + unit * 2560 + w * 512, o);
    }
    STAMP(11);
  } else if (isM && mu >= 46) {
    const int ru = mu >= 50 ? mu - 50 : mu - 40;
    if (mu < 50) {
      BAR();
      const rsrc_t r = mk(wbase + row0 * 7680 + (50 + ru) * 128, 80 * 7680 - (50 + ru) * 128);
#pragma unroll
      for (int i = 0; i < 10; ++i) W[i] = ld4<NT>(r, wvo, i * 7680);
      SB();
#pragma unroll
      for (int j = 0; j < 5; ++j) pair_rows(W[2 * j], W[2 * j + 1], W[2 * j], W[2 * j + 1]);
    }
    u32 s[6];
    spread(pk(silu_b(lo_(ap)), silu_b(hi_(ap))), s);
    *(float*)(lds + L_R2 + w * 256 + tile_col(l) * 4) = tile_dot<5>(W, s, l);
    BAR();
    if (w == 0) {
      float gb = rb(col_sum(lds + L_R2 + l * 4));
      const u32 xw = ld1<0>(mk(P.x, 1280), (l >> 1) * 4, ru * 128);
      v2u gg;
      for (u32 spin = 0; spin < SPIN_CAP; ++spin) {
        CFENCE();
        gg = ld2<SC1>(rws, (l >> 1) * 8, WS_MOD + 2 * 2560 + ru * 256);
        if (!any(gg.y != tag)) break;
      }
      u32 a[8];
      for (u32 spin = 0; spin < SPIN_CAP; ++spin) {
        CFENCE();
#pragma unroll
        for (int g = 0; g < 8; ++g) a[g] = ld1<SC1>(rws, l * 4, WS_AB + g * 2560 + ru * 256);
        if (!any(umax(umax(umax(a[0], a[1]), umax(a[2], a[3])), umax(umax(a[4], a[5]), umax(a[6], a[7]))) == SENT)) break;
      }
      const float sm = ((f_(a[0]) + f_(a[1])) + (f_(a[2]) + f_(a[3]))) + ((f_(a[4]) + f_(a[5])) + (f_(a[6]) + f_(a[7])));
      float x1 = rb(((l & 1) ? hi_(xw) : lo_(xw)) + rb(((l & 1) ? hi_(gg.x) : lo_(gg.x)) * rb(sm)));
      const int src = ((l & 15) * 4 + (l >> 5) * 2 + ((l >> 4) & 1)) * 4;
      x1 = f_(__builtin_amdgcn_ds_bpermute(src, u_(x1))); gb = f_(__builtin_amdgcn_ds_bpermute(src, u_(gb)));
      PIN(x1); PIN(gb);
      STAMP(9);
      v4u c[6];
      const int co = (l >> 4) * 2560 + (l & 15) * 16;
      for (u32 spin = 0; spin < SPIN_CAP; ++spin) {
        CFENCE();
        u32 top = 0;
#pragma unroll
        for (int i = 0; i < 6; ++i) c[i] = ld4<SC1>(rws, co, WS_FC + i * 4 * 2560 + ru * 256);
#pragma unroll
        for (int i = 0; i < 6; ++i) top = umax(top, top4(c[i]));
        if (!any(top == SENT)) break;
      }
      STAMP(10);
      const v4f y = ((__builtin_bit_cast(v4f, c[0]) + __builtin_bit_cast(v4f, c[1])) + (__builtin_bit_cast(v4f, c[2]) + __builtin_bit_cast(v4f, c[3]))) +
                    (__builtin_bit_cast(v4f, c[4]) + __builtin_bit_cast(v4f, c[5]));
      const float ffn = sw16(sw32(y[0], y[2]), sw32(y[1], y[3]));
      const __bf16 res = (__bf16)(x1 + rb(gb * rb(ffn)));
      __builtin_amdgcn_raw_buffer_store_b16(__builtin_bit_cast(unsigned short, res), mk(P.out, 1280), src >> 1, ru * 128, 0);
      __builtin_amdgcn_raw_buffer_store_b64(v2u{tag, (u32)(tag64 >> 32)}, rws, (ru == 0 && l == 0) ? WS_E : OOB, 0, SC1);
      STAMP(11);
    }
  }
}
__global__ void code_end_marker() { asm volatile(".Lcode_end:"); }

extern "C" void kernel_launch(void* const* d_in, const int* in_sizes, int n_in, void* d_out, int out_size, void* d_ws,
                              size_t ws_size, hipStream_t stream) {
  Params P;
  P.x = d_in[0]; P.kc = d_in[1]; P.vc = d_in[2]; P.wq = d_in[3]; P.wo = d_in[4];
  P.wg = d_in[6]; P.wu = d_in[7]; P.wd = d_in[8]; P.c = d_in[9]; P.wmod = d_in[10];
  P.out = d_out; P.ws = d_ws;
  fused_block<<<256, 512, 0, stream>>>(P);
}
```

```cpp
#define DIAG
#define PROBE 100
#define PMODE 0
#include <hip/hip_runtime.h>
#include <stdint.h>

typedef unsigned u32;
typedef unsigned long long u64;
typedef u32 v4u __attribute__((ext_vector_type(4)));
typedef u32 v3u __attribute__((ext_vector_type(3)));
typedef u32 v2u __attribute__((ext_vector_type(2)));
typedef float v4f __attribute__((ext_vector_type(4)));
typedef float v2f __attribute__((ext_vector_type(2)));
typedef __bf16 bf16x2 __attribute__((ext_vector_type(2)));
typedef __amdgpu_buffer_rsrc_t rsrc_t;
#define DEV __device__ __forceinline__
#define UNLIKELY(x) (!(ABL & 1) && __builtin_expect(!!(x), 0))
#define LAS __attribute__((address_space(3)))

#define PWK 3
#define PWKD 5
#define PWV 4
#define PWVD 6
#define NAHEAD 24
#define KFIRST 2
#define HPOS 9
#define QPOS 22
#define VQK 4
#define STPOS 18
#define CODEPF 1
#define ABL 0

constexpr int D = 640, DH = 192, SK = 32768, WIN = 4096;
constexpr int NT = 2, SC1 = 16;
constexpr u32 SENT = 0xFFFFFFFFu;
constexpr u32 SPIN_CAP = (ABL & 1) ? 1 : 1u << 16;
constexpr int WS_E = 0, WS_MOD = 1024, WS_Q = 16384, WS_ST = 40960, WS_OA = 45056, WS_AB = WS_OA + 98304, WS_FC = WS_AB + 20480,
              WS_END = WS_FC + 61440;
constexpr int L_CNT = 0, L_ST = 64, L_Q = 128, L_RED = 512, L_R2 = L_RED + 8 * 768, L_G = L_R2 + 4096, L_END = L_G + 1024;

struct Params {
  const void* kc; const void* vc; void* ws; const void* wq; const void* wo; const void* wg; const void* wmod; const void* c;
  const void* x; const void* wu; const void* wd; void* out;
};

DEV rsrc_t mk(const void* p, u32 bytes) { return __builtin_amdgcn_make_buffer_rsrc((void*)p, 0, bytes, 0x00020000); }
template <int AUX> DEV v4u ld4(rsrc_t r, int v, int s) { return __builtin_amdgcn_raw_buffer_load_b128(r, v, s, AUX); }
template <int AUX> DEV v3u ld3(rsrc_t r, int v, int s) { return __builtin_amdgcn_raw_buffer_load_b96(r, v, s, AUX); }
template <int AUX> DEV v2u ld2(rsrc_t r, int v, int s) { return __builtin_amdgcn_raw_buffer_load_b64(r, v, s, AUX); }
template <int AUX> DEV u32 ld1(rsrc_t r, int v, int s) { return __builtin_amdgcn_raw_buffer_load_b32(r, v, s, AUX); }
DEV void st4(rsrc_t r, int v, int s, v4u d) { __builtin_amdgcn_raw_buffer_store_b128(d, r, v, s, SC1); }
DEV float f_(u32 x) { return __builtin_bit_cast(float, x); }
DEV u32 u_(float x) { return __builtin_bit_cast(u32, x); }
DEV float lo_(u32 x) { return f_(x << 16); }
DEV float hi_(u32 x) { return f_(x & 0xffff0000u); }
DEV u32 pk(float a, float b) { bf16x2 t = {(__bf16)a, (__bf16)b}; return __builtin_bit_cast(u32, t); }
DEV float rb(float a) { return (float)(__bf16)a; }
DEV float rcp(float a) { return __builtin_amdgcn_rcpf(a); }
DEV u32 nz(u32 x) { return x < 0xFFFFFFFEu ? x : 0xFFFFFFFEu; }
DEV u32 umax(u32 a, u32 b) { return a > b ? a : b; }
DEV u32 top4(v4u x) { return umax(umax(x.x, x.y), umax(x.z, x.w)); }
DEV bool any(bool c) { return __builtin_amdgcn_ballot_w64(c) != 0; }
DEV float dot2(u32 a, u32 b, float c) {
  return __builtin_amdgcn_fdot2_f32_bf16(__builtin_bit_cast(bf16x2, a), __builtin_bit_cast(bf16x2, b), c, false);
}
template <int CTRL> DEV float dpp(float x) { return f_(__builtin_amdgcn_mov_dpp(u_(x), CTRL, 0xf, 0xf, true)); }
template <int CTRL> DEV u32 dppu(u32 x) { return __builtin_amdgcn_mov_dpp(x, CTRL, 0xf, 0xf, true); }
constexpr int X1 = 0xB1, X2 = 0x4E, X3 = 0x1B, HM = 0x141, R8 = 0x128;
template <int AND, int OR> DEV u32 swz(u32 x) { return __builtin_amdgcn_ds_swizzle(x, AND | (OR << 5)); }
DEV float sw32(float a, float b) {
  auto t = __builtin_amdgcn_permlane32_swap(u_(a), u_(b), false, false); return f_(t[0]) + f_(t[1]);
}
DEV float sw16(float a, float b) {
  auto t = __builtin_amdgcn_permlane16_swap(u_(a), u_(b), false, false); return f_(t[0]) + f_(t[1]);
}
DEV float mx32(float a) { auto t = __builtin_amdgcn_permlane32_swap(u_(a), u_(a), false, false); return fmaxf(f_(t[0]), f_(t[1])); }
DEV float mx16(float a) { auto t = __builtin_amdgcn_permlane16_swap(u_(a), u_(a), false, false); return fmaxf(f_(t[0]), f_(t[1])); }
DEV float sum8(float a) { a += dpp<X1>(a); a += dpp<X2>(a); a += dpp<HM>(a); return a; }
DEV float sumhi(float a) { a += dpp<R8>(a); a = sw16(a, a); return sw32(a, a); }
DEV float max8(float a) { a = fmaxf(a, dpp<X1>(a)); a = fmaxf(a, dpp<X2>(a)); return fmaxf(a, dpp<HM>(a)); }
DEV float maxhi(float a) { a = fmaxf(a, dpp<R8>(a)); return mx32(mx16(a)); }
DEV float sum64(float a) { return sumhi(sum8(a)); }
DEV float ex(float a) { return __builtin_amdgcn_exp2f(a * 1.4426950408889634f); }
DEV u32 plo(u32 a, u32 b) { return __builtin_amdgcn_perm(b, a, 0x05040100u); }
DEV u32 phi(u32 a, u32 b) { return __builtin_amdgcn_perm(b, a, 0x07060302u); }
#define SB() __builtin_amdgcn_sched_barrier(0)
#define PACE2(deep, n) do { asm volatile("s_waitcnt vmcnt(%1)\n\ts_cmp_lt_u32 %0, %3\n\ts_cbranch_scc1 1f\n\ts_waitcnt vmcnt(%2)\n1:" \
                              :: "s"(ahead), "n"((deep) - 1), "n"((n) - 1), "n"(NAHEAD) : "memory", "scc"); SB(); } while (0)
#define LWAIT() asm volatile("s_waitcnt lgkmcnt(0)" ::: "memory")
#define BAR() do { asm volatile("s_waitcnt lgkmcnt(0)\n\ts_barrier" ::: "memory"); SB(); } while (0)
#define CFENCE() asm volatile("" ::: "memory")
#define PIN(x) asm volatile("" : "+v"(x))
constexpr int OOB = 0x7ffffff0;

#define STAMP(i) do { SB(); asm volatile("s_memrealtime %0\n\ts_waitcnt lgkmcnt(0)" : "=s"(ts[i]) :: "memory"); SB(); } while (0)

DEV void pair_rows(const v4u a, const v4u b, v4u& p0, v4u& p1) {
  p0 = v4u{plo(a.x, b.x), phi(a.x, b.x), plo(a.y, b.y), phi(a.y, b.y)};
  p1 = v4u{plo(a.z, b.z), phi(a.z, b.z), plo(a.w, b.w), phi(a.w, b.w)};
  PIN(p0); PIN(p1);
}
template <int J> DEV u32 bc8(u32 a) { return swz<0x18, J>(a); }
DEV void spread(u32 a, u32 (&s)[6]) { s[0] = bc8<0>(a); s[1] = bc8<1>(a); s[2] = bc8<2>(a); s[3] = bc8<3>(a); s[4] = bc8<4>(a); s[5] = bc8<5>(a); }
template <int N> DEV float tile_dot(const v4u (&W)[12], const u32 (&s)[6], int l) {
  float a[8];
#pragma unroll
  for (int c = 0; c < 8; ++c) a[c] = 0.f;
#pragma unroll
  for (int j = 0; j < N; ++j)
#pragma unroll
    for (int c = 0; c < 8; ++c) a[c] = dot2(W[2 * j + (c >> 2)][c & 3], s[j], a[c]);
  const float y0 = sw32(a[0], a[4]), y1 = sw32(a[1], a[5]), y2 = sw32(a[2], a[6]), y3 = sw32(a[3], a[7]);
  float z0 = sw16(y0, y2), z1 = sw16(y1, y3);
  z0 += dpp<R8>(z0); z1 += dpp<R8>(z1);
  return (l & 8) ? z1 : z0;
}
DEV int tile_col(int l) { return (l & 7) * 8 + (l >> 5) * 4 + ((l >> 4) & 1) * 2 + ((l >> 3) & 1); }
DEV float col_sum(const char* p) {
  const float* f = (const float*)p;
  return ((f[0] + f[64]) + (f[128] + f[192])) + ((f[256] + f[320]) + (f[384] + f[448]));
}
DEV bool last_of_eight(char* lds, int which) {
  LWAIT();
  u32 old = 0;
  if ((threadIdx.x & 63) == 0) old = __hip_atomic_fetch_add((LAS u32*)(lds + L_CNT) + which, 1u, __ATOMIC_RELAXED, __HIP_MEMORY_SCOPE_WORKGROUP);
  const bool last = __builtin_amdgcn_readfirstlane(old) == 7;
  CFENCE();
  return last;
}
DEV float silu_b(float v) { return v * rcp(1.f + ex(-v)); }

__global__ void __launch_bounds__(512, 2) fused_block(Params P) {
  __shared__ __attribute__((aligned(16))) char lds[L_END];
  u64 ts[16];
#pragma unroll
  for (int i = 0; i < 16; ++i) ts[i] = 0;
  STAMP(0);
  const int b = blockIdx.x, l = threadIdx.x & 63, w = __builtin_amdgcn_readfirstlane(threadIdx.x >> 6);
  const int xg = b & 7, idx = b >> 3, head = 4 * xg + (idx >> 3), sp = idx & 7;
  const int r8 = l >> 3, c8 = l & 7;
  const size_t kvbase = ((size_t)head * SK + (SK - WIN) + sp * 512 + w * 64) * (DH * 2);
  const rsrc_t rk = mk((const char*)P.kc + kvbase, 64 * 384);
  const int kvo = r8 * 384 + c8 * 16;
  v4u K[24];
#pragma unroll
  for (int i = 0; i < KFIRST; ++i) K[i] = ld4<NT>(rk, kvo + (i % 3) * 128, (i / 3) * 3072);
  SB();
  u32 cpf = 0;
  {
    u64 pc; u32 len;
    asm volatile("s_getpc_b64 %0\n.Lcode_begin:\n\ts_mov_b32 %1, .Lcode_end-.Lcode_begin" : "=s"(pc), "=s"(len));
    cpf = ld1<0>(mk((const void*)pc, len), l == 0 ? (idx * 8 + w) * 128 : OOB, 0);
    SB();
  }
  const bool isQ = idx < 12, isO = idx >= 12 && idx < 22, isG = idx >= 22 && idx < 25, isM = idx >= 25;
  const int mu = (idx - 25) * 8 + xg;
  const int unit = isQ ? 12 * xg + idx : isO ? idx - 12 : isG ? 3 * xg + idx - 22 : mu;
  const int stride = isQ ? 12288 : isO ? 1280 : isG ? 3072 : 7680;
  const int nrow = isO ? 12 : 10;
  const int row0 = (isO ? 768 * xg : 0) + w * 8 * nrow;
  const char* wbase = (const char*)(isQ ? P.wq : isO ? P.wo : isG ? P.wg : P.wmod);
  const int woff = row0 * stride + unit * 128, wlen = 8 * nrow * stride - unit * 128;
  const rsrc_t rw = mk(wbase + woff, (ABL & 4) ? 0 : wlen);
  const int wvo = r8 * nrow * stride + c8 * 16;
  const rsrc_t rws = mk(P.ws, (ABL & 2) ? 0 : WS_END);
  const rsrc_t rz = mk(P.ws, 0);
#define RW(bit) ((ABL & (bit)) ? rz : rws)
  const u32 ahead = b - 176;
  const u64 ecall = *(const __attribute__((address_space(4))) u64*)P.ws;
  const u64 tag64 = ecall * 6364136223846793005ull + 1442695040888963407ull;
  const u32 tag = (u32)tag64;

  if (isM && mu < 20) {
    const v4u e = {SENT, SENT, SENT, SENT};
    st4(RW(8), l * 16, WS_OA + (mu * 8 + w) * 1024, e);
    st4(RW(8), l * 16, WS_OA + (160 + mu * 8 + w) * 1024, e);
  }
  const int pe = w * 80 + r8 * 10 + (c8 < 5 ? c8 : 4) * 2;
  const u32 ap = ld1<0>(mk(isM ? P.c : P.x, 1280), pe * 2, 0);
  v4u xa; u32 xb;
  if (isQ) { xa = ld4<0>(mk(P.x, 1280), l * 20, 0); xb = ld1<0>(mk(P.x, 1280), l * 20 + 16, 0); }
  if (threadIdx.x == 0) *(v2u*)(lds + L_CNT) = v2u{0u, 0u};
  SB();
  v4u W[12];
#pragma unroll
  for (int i = 0; i < 10; ++i) {
    W[i] = ld4<NT>(rw, wvo, i * stride);
    SB();
    if (i == 0) BAR();
    if (KFIRST + i >= PWK - 1) PACE2(PWKD, PWK);
  }
  auto kload = [&](int i) { K[i] = ld4<NT>(rk, kvo + (i % 3) * 128, (i / 3) * 3072); SB(); PACE2(PWKD, PWK); };
  constexpr int MPOS = KFIRST + PWK;
#pragma unroll
  for (int i = KFIRST; i < MPOS; ++i) kload(i);
#pragma unroll
  for (int j = 0; j < 5; ++j) pair_rows(W[2 * j], W[2 * j + 1], W[2 * j], W[2 * j + 1]);
  SB();
  STAMP(1);
  auto publish_tagged = [&](int base) {
    const float v = col_sum(lds + L_R2 + l * 4);
    const u32 p0 = pk(v, dpp<X1>(v)), p1 = dppu<X2>(p0);
    st4(RW(64), (l & 3) == 0 ? l * 4 : OOB, base, v4u{p0, tag, p1, tag});
  };
  float ln0 = 0.f, ln1 = 0.f;
  if (isM && mu < 50) {
    u32 s[6];
    spread(pk(silu_b(lo_(ap)), silu_b(hi_(ap))), s);
    *(float*)(lds + L_R2 + w * 256 + tile_col(l) * 4) = tile_dot<5>(W, s, l);
    if (last_of_eight(lds, 0)) publish_tagged(WS_MOD + mu * 256);
    SB();
    STAMP(2);
  } else if (isQ) {
    float v[10] = {lo_(xa.x), hi_(xa.x), lo_(xa.y), hi_(xa.y), lo_(xa.z), hi_(xa.z), lo_(xa.w), hi_(xa.w), lo_(xb), hi_(xb)};
    float s = 0.f, q = 0.f;
#pragma unroll
    for (int i = 0; i < 10; ++i) s += v[i];
    const float mean = sum64(s) * (1.f / D);
#pragma unroll
    for (int i = 0; i < 10; ++i) { const float d = v[i] - mean; q = fmaf(d, d, q); }
    const float var = sum64(q) * (1.f / D);
    const float m = rb(mean), rs = rcp(rb(__builtin_amdgcn_sqrtf(rb(rb(var) + 1e-5f))));
    ln0 = rb(rb(lo_(ap) - m) * rs); ln1 = rb(rb(hi_(ap) - m) * rs);
    PIN(ln0); PIN(ln1);
    SB();
  }
#pragma unroll
  for (int i = MPOS; i < HPOS; ++i) kload(i);
  v2u gs, gc;
  if (isQ) { gs = ld2<SC1>(RW(128), pe * 4, WS_MOD); gc = ld2<SC1>(RW(128), pe * 4, WS_MOD + 2560); SB(); }
#pragma unroll
  for (int i = HPOS; i < HPOS + PWK; ++i) kload(i);
  if (isQ) {
    if (UNLIKELY(any((gs.y != tag) | (gc.y != tag)))) {
      for (u32 spin = 0; spin < SPIN_CAP; ++spin) {
        CFENCE();
        gs = ld2<SC1>(rws, pe * 4, WS_MOD); gc = ld2<SC1>(rws, pe * 4, WS_MOD + 2560);
        if (!any((gs.y != tag) | (gc.y != tag))) break;
      }
    }
    STAMP(2);
    u32 s[6];
    spread(pk(rb(ln0 * rb(1.f + lo_(gc.x))) + lo_(gs.x), rb(ln1 * rb(1.f + hi_(gc.x))) + hi_(gs.x)), s);
    *(float*)(lds + L_R2 + w * 256 + tile_col(l) * 4) = tile_dot<5>(W, s, l);
    if (last_of_eight(lds, 0)) publish_tagged(WS_Q + unit * 256);
    SB();
    STAMP(3);
  }
#pragma unroll
  for (int i = HPOS + PWK; i < QPOS; ++i) kload(i);
  const int qo = (l < 48 ? l : 0) * 16;
  v4u qg = ld4<SC1>(RW(32), qo, WS_Q + head * 768);
  SB();
#pragma unroll
  for (int i = QPOS; i < 24; ++i) kload(i);
  const rsrc_t rv = mk((const char*)P.vc + kvbase, 64 * 384);
  const int vvo = (l >> 5) * 4 * 384 + (l & 31) * 12;
  v3u V[32];
  auto vload = [&](int i) { V[i] = ld3<NT>(rv, vvo + (i & 3) * 384, (i >> 2) * 3072); SB(); PACE2(PWVD, PWV); };
#pragma unroll
  for (int i = 0; i < VQK; ++i) vload(i);
  asm volatile("" :: "v"(cpf));
  STAMP(4);
  if (UNLIKELY(any((qg.y != tag) | (qg.w != tag)))) {
    for (u32 spin = 0; spin < SPIN_CAP; ++spin) {
      CFENCE();
      qg = ld4<SC1>(rws, qo, WS_Q + head * 768);
      if (!any((qg.y != tag) | (qg.w != tag))) break;
    }
  }
  STAMP(5);
  float p[8], mw;
  {
    *(v2u*)(lds + L_Q + (qo >> 1)) = v2u{qg.x, qg.z};
    LWAIT();
    v4u qf[3];
#pragma unroll
    for (int k = 0; k < 3; ++k) qf[k] = *(const v4u*)(lds + L_Q + k * 128 + c8 * 16);
    float m = -INFINITY;
#pragma unroll
    for (int t = 0; t < 8; ++t) {
      float s = 0.f;
#pragma unroll
      for (int k = 0; k < 3; ++k)
#pragma unroll
        for (int d = 0; d < 4; ++d) s = dot2(K[3 * t + k][d], qf[k][d], s);
      s = rb(rb(sum8(s)) * 0.07216878364870322f);
      p[t] = s; m = fmaxf(m, s);
      if (t == 3) { PIN(p[0]); PIN(p[1]); PIN(p[2]); PIN(p[3]); SB(); vload(VQK); }
    }
    mw = maxhi(m);
    float ls = 0.f;
#pragma unroll
    for (int t = 0; t < 8; ++t) { p[t] = ex(p[t] - mw); ls += p[t]; }
    ls = sumhi(ls);
    *(v2f*)(lds + L_ST + w * 8) = v2f{mw, ls};
    SB();
    vload(VQK + 1);
    if (last_of_eight(lds, 1)) {
      const v2f t = *(const v2f*)(lds + L_ST + c8 * 8);
      const float mc = max8(t[0]), lc = sum8(t[1] * ex(t[0] - mc));
      st4(RW(16), l == 0 ? 0 : OOB, WS_ST + (head * 8 + sp) * 16, v4u{u_(mc), tag, u_(lc), tag});
    }
    SB();
  }
  STAMP(14);
  v2f acc[3] = {{0.f, 0.f}, {0.f, 0.f}, {0.f, 0.f}};
  float pb[32];
  auto pspread = [&](int t) {
    pb[4 * t] = f_(swz<0, 0>(u_(p[t]))); pb[4 * t + 1] = f_(swz<0, 8>(u_(p[t])));
    pb[4 * t + 2] = f_(swz<0, 16>(u_(p[t]))); pb[4 * t + 3] = f_(swz<0, 24>(u_(p[t])));
    if (t == 7) { PIN(pb[28]); PIN(pb[29]); PIN(pb[30]); PIN(pb[31]); }
  };
  auto pv = [&](int i) {
    if ((i & 3) == 0 && i < 28) pspread(i >> 2);
    const v2f pp = {pb[i], pb[i]};
#pragma unroll
    for (int d = 0; d < 3; ++d) acc[d] = __builtin_elementwise_fma(pp, v2f{lo_(V[i][d]), hi_(V[i][d])}, acc[d]);
    asm volatile("" : "+v"(acc[0]), "+v"(acc[1]), "+v"(acc[2]));
    SB();
  };
  v4u st;
  constexpr int STCHK = 28;
  static_assert(STPOS + PWVD <= 31, "statistics must have landed");
  constexpr int LAG = PWVD - 1, FIRST = VQK + 2;
#pragma unroll
  for (int t = 0; t < FIRST - LAG; ++t) pv(t);
#pragma unroll
  for (int i = FIRST; i < 32; ++i) {
    if (i == STPOS) { st = ld4<SC1>(RW(256), c8 * 16, WS_ST + head * 128); SB(); }
    vload(i);
    if (i - LAG >= 0 && i - LAG < STCHK) pv(i - LAG);
  }
  constexpr int done = 32 - LAG < STCHK ? 32 - LAG : STCHK;
  {
    const rsrc_t r2 = mk(wbase + woff, isO ? wlen : 0);
    W[10] = ld4<NT>(r2, wvo, 10 * stride); W[11] = ld4<NT>(r2, wvo, 11 * stride);
    SB();
  }
  STAMP(6);
  if (UNLIKELY(any((st.y != tag) | (st.w != tag)))) {
    for (u32 spin = 0; spin < SPIN_CAP; ++spin) {
      CFENCE();
      st = ld4<SC1>(rws, c8 * 16, WS_ST + head * 128);
      if (!any((st.y != tag) | (st.w != tag))) break;
    }
  }
  const float M = max8(f_(st.x));
  const float scale = ex(mw - M) * rcp(sum8(f_(st.z) * ex(f_(st.x) - M)));
  p[7] *= scale;
  pspread(7);
  SB();
#pragma unroll
  for (int t = done; t < STCHK; ++t) pv(t);
#pragma unroll
  for (int d = 0; d < 3; ++d) acc[d] *= scale;
#pragma unroll
  for (int t = STCHK; t < 32; ++t) pv(t);
  STAMP(7);
  {
    float* dst = (float*)(lds + L_RED + w * 768 + (l & 31) * 24 + (l >> 5) * 12);
    dst[0] = sw32(acc[0][0], acc[1][1]); dst[1] = sw32(acc[0][1], acc[2][0]); dst[2] = sw32(acc[1][0], acc[2][1]);
  }
  BAR();
  if (w == 7) {
    const char* src = lds + L_RED + qo;
    v4f a = *(const v4f*)src;
#pragma unroll
    for (int i = 1; i < 8; ++i) a += *(const v4f*)(src + i * 768);
    const u32 d0 = nz(pk(a[0], a[1])), d1 = nz(pk(a[2], a[3]));
    const u32 e0 = dppu<X1>(d0), e1 = dppu<X1>(d1);
    st4(rws, (l < 48 && !(l & 1)) ? l * 8 : OOB, WS_OA + (head * 8 + sp) * 384, v4u{d0, d1, e0, e1});
  }
  SB();
  STAMP(8);

  if (isO) {
    pair_rows(W[10], W[11], W[10], W[11]);
    u32 one_lo = 0x00003f80u, one_hi = 0x3f800000u;
    PIN(one_lo); PIN(one_hi);
    SB();
    STAMP(9);
    u32 a[8];
    const int ao = (r8 * 6 + (c8 < 6 ? c8 : 5)) * 4, as = WS_OA + (4 * xg + (w >> 1)) * 3072 + (w & 1) * 192;
    for (u32 spin = 0; spin < SPIN_CAP; ++spin) {
      CFENCE();
#pragma unroll
      for (int s = 0; s < 8; ++s) a[s] = ld1<SC1>(rws, ao + s * 384, as);
      if (!any(umax(umax(umax(a[0], a[1]), umax(a[2], a[3])), umax(umax(a[4], a[5]), umax(a[6], a[7]))) == SENT)) break;
    }
    STAMP(10);
    float o0 = 0.f, o1 = 0.f, o2 = 0.f, o3 = 0.f;
#pragma unroll
    for (int s = 0; s < 4; ++s) {
      o0 = dot2(a[s], one_lo, o0); o1 = dot2(a[s], one_hi, o1);
      o2 = dot2(a[4 + s], one_lo, o2); o3 = dot2(a[4 + s], one_hi, o3);
    }
    u32 s[6];
    spread(pk(o0 + o2, o1 + o3), s);
    *(float*)(lds + L_R2 + w * 256 + tile_col(l) * 4) = tile_dot<6>(W, s, l);
    BAR();
    if (w == 0) {
      const u32 v = nz(u_(col_sum(lds + L_R2 + l * 4)));
      st4(rws, (l & 3) == 0 ? l * 4 : OOB, WS_AB + xg * 2560 + unit * 256, v4u{v, dppu<X1>(v), dppu<X2>(v), dppu<X3>(v)});
    }
    STAMP(11);
  } else if (isG) {
    BAR();
    v4u U[12];
    v2u Dn[32];
    const rsrc_t ru = mk((const char*)P.wu + woff, wlen);
#pragma unroll
    for (int i = 0; i < 10; ++i) U[i] = ld4<NT>(ru, wvo, i * 3072);
    v2u gg, gb2, gc2;
    gg = ld2<SC1>(rws, pe * 4, WS_MOD + 2 * 2560); gb2 = ld2<SC1>(rws, pe * 4, WS_MOD + 3 * 2560); gc2 = ld2<SC1>(rws, pe * 4, WS_MOD + 4 * 2560);
    const rsrc_t rd = mk((const char*)P.wd + unit * 64 * 1280, w < 5 ? 64 * 1280 : 0);
#pragma unroll
    for (int i = 0; i < 32; ++i) Dn[i] = ld2<NT>(rd, (l >> 5) * 32 * 1280 + (l & 31) * 8, i * 1280 + w * 256);
    SB();
    if (UNLIKELY(any((gg.y != tag) | (gb2.y != tag) | (gc2.y != tag)))) {
      for (u32 spin = 0; spin < SPIN_CAP; ++spin) {
        CFENCE();
        gg = ld2<SC1>(rws, pe * 4, WS_MOD + 2 * 2560); gb2 = ld2<SC1>(rws, pe * 4, WS_MOD + 3 * 2560); gc2 = ld2<SC1>(rws, pe * 4, WS_MOD + 4 * 2560);
        if (!any((gg.y != tag) | (gb2.y != tag) | (gc2.y != tag))) break;
      }
    }
#pragma unroll
    for (int j = 0; j < 5; ++j) pair_rows(U[2 * j], U[2 * j + 1], U[2 * j], U[2 * j + 1]);
    v4u Dp[16];
#pragma unroll
    for (int j = 0; j < 16; ++j) {
      Dp[j] = v4u{plo(Dn[2 * j].x, Dn[2 * j + 1].x), phi(Dn[2 * j].x, Dn[2 * j + 1].x), plo(Dn[2 * j].y, Dn[2 * j + 1].y), phi(Dn[2 * j].y, Dn[2 * j + 1].y)};
      PIN(Dp[j]);
    }
    float ga0 = lo_(gg.x), ga1 = hi_(gg.x), sb0 = lo_(gb2.x), sb1 = hi_(gb2.x), cb0 = rb(1.f + lo_(gc2.x)), cb1 = rb(1.f + hi_(gc2.x));
    float xv0 = lo_(ap), xv1 = hi_(ap), live = c8 < 5 ? 1.f : 0.f;
    PIN(ga0); PIN(ga1); PIN(sb0); PIN(sb1); PIN(cb0); PIN(cb1); PIN(xv0); PIN(xv1); PIN(live);
    SB();
    STAMP(9);
    v2u a[8];
    for (u32 spin = 0; spin < SPIN_CAP; ++spin) {
      CFENCE();
      u32 top = 0;
#pragma unroll
      for (int g = 0; g < 8; ++g) a[g] = ld2<SC1>(rws, pe * 4, WS_AB + g * 2560);
#pragma unroll
      for (int g = 0; g < 8; ++g) top = umax(top, umax(a[g].x, a[g].y));
      if (!any(top == SENT)) break;
    }
    STAMP(10);
    const v2f sm = ((__builtin_bit_cast(v2f, a[0]) + __builtin_bit_cast(v2f, a[1])) + (__builtin_bit_cast(v2f, a[2]) + __builtin_bit_cast(v2f, a[3]))) +
                   ((__builtin_bit_cast(v2f, a[4]) + __builtin_bit_cast(v2f, a[5])) + (__builtin_bit_cast(v2f, a[6]) + __builtin_bit_cast(v2f, a[7])));
    const float x0 = rb(xv0 + rb(ga0 * rb(sm[0]))), x1 = rb(xv1 + rb(ga1 * rb(sm[1])));
    float t = sw32((x0 + x1) * live, fmaf(x0, x0, x1 * x1) * live);
    t = sum8(t); t += dpp<R8>(t); t = sw16(t, t);
    *(float*)(lds + L_ST + (l >> 5) * 32 + w * 4) = t;
    BAR();
    const v4f s0 = *(const v4f*)(lds + L_ST), s1 = *(const v4f*)(lds + L_ST + 16), q0 = *(const v4f*)(lds + L_ST + 32), q1 = *(const v4f*)(lds + L_ST + 48);
    const v4f ss = s0 + s1, qq = q0 + q1;
    const float mean = ((ss[0] + ss[1]) + (ss[2] + ss[3])) * (1.f / D);
    const float var = fmaxf(fmaf(-mean, mean, ((qq[0] + qq[1]) + (qq[2] + qq[3])) * (1.f / D)), 0.f);
    const float m = rb(mean), rs = rcp(rb(__builtin_amdgcn_sqrtf(rb(rb(var) + 1e-5f))));
    u32 s[6];
    spread(pk(rb(rb(rb(x0 - m) * rs) * cb0) + sb0, rb(rb(rb(x1 - m) * rs) * cb1) + sb1), s);
    const float tg = tile_dot<5>(W, s, l), tu = tile_dot<5>(U, s, l);
    *(float*)(lds + L_R2 + w * 256 + tile_col(l) * 4) = tg;
    *(float*)(lds + L_R2 + 2048 + w * 256 + tile_col(l) * 4) = tu;
    BAR();
    if (w < 5) {
      const float gt = rb(col_sum(lds + L_R2 + l * 4)), up = rb(col_sum(lds + L_R2 + 2048 + l * 4));
      const float u = 0.7978845608028654f * (gt + 0.044715f * gt * gt * gt);
      const float th = 1.f - 2.f * rcp(1.f + ex(2.f * u));
      const float g = rb(0.5f * gt * (1.f + th)) * up;
      *(__bf16*)(lds + L_G + w * 128 + l * 2) = (__bf16)g;
      LWAIT();
      v4u gp[4];
#pragma unroll
      for (int i = 0; i < 4; ++i) gp[i] = *(const v4u*)(lds + L_G + w * 128 + (l >> 5) * 64 + i * 16);
      float y[4] = {0.f, 0.f, 0.f, 0.f};
#pragma unroll
      for (int j = 0; j < 16; ++j)
#pragma unroll
        for (int c = 0; c < 4; ++c) y[c] = dot2(Dp[j][c], gp[j >> 2][j & 3], y[c]);
      v4u o;
#pragma unroll
      for (int c = 0; c < 4; ++c) o[c] = nz(u_(sw32(y[c], y[c])));
      st4(rws, l < 32 ? l * 16 : OOB, WS_FC + unit * 2560 + w * 512, o);
    }
    STAMP(11);
  } else if (isM && mu >= 46) {
    const int ru = mu >= 50 ? mu - 50 : mu - 40;
    if (mu < 50) {
      BAR();
      const rsrc_t r = mk(wbase + row0 * 7680 + (50 + ru) * 128, 80 * 7680 - (50 + ru) * 128);
#pragma unroll
      for (int i = 0; i < 10; ++i) W[i] = ld4<NT>(r, wvo, i * 7680);
      SB();
#pragma unroll
      for (int j = 0; j < 5; ++j) pair_rows(W[2 * j], W[2 * j + 1], W[2 * j], W[2 * j + 1]);
    }
    u32 s[6];
    spread(pk(silu_b(lo_(ap)), silu_b(hi_(ap))), s);
    *(float*)(lds + L_R2 + w * 256 + tile_col(l) * 4) = tile_dot<5>(W, s, l);
    BAR();
    if (w == 0) {
      float gb = rb(col_sum(lds + L_R2 + l * 4));
      const u32 xw = ld1<0>(mk(P.x, 1280), (l >> 1) * 4, ru * 128);
      v2u gg;
      for (u32 spin = 0; spin < SPIN_CAP; ++spin) {
        CFENCE();
        gg = ld2<SC1>(rws, (l >> 1) * 8, WS_MOD + 2 * 2560 + ru * 256);
        if (!any(gg.y != tag)) break;
      }
      u32 a[8];
      for (u32 spin = 0; spin < SPIN_CAP; ++spin) {
        CFENCE();
#pragma unroll
        for (int g = 0; g < 8; ++g) a[g] = ld1<SC1>(rws, l * 4, WS_AB + g * 2560 + ru * 256);
        if (!any(umax(umax(umax(a[0], a[1]), umax(a[2], a[3])), umax(umax(a[4], a[5]), umax(a[6], a[7]))) == SENT)) break;
      }
      const float sm = ((f_(a[0]) + f_(a[1])) + (f_(a[2]) + f_(a[3]))) + ((f_(a[4]) + f_(a[5])) + (f_(a[6]) + f_(a[7])));
      float x1 = rb(((l & 1) ? hi_(xw) : lo_(xw)) + rb(((l & 1) ? hi_(gg.x) : lo_(gg.x)) * rb(sm)));
      const int src = ((l & 15) * 4 + (l >> 5) * 2 + ((l >> 4) & 1)) * 4;
      x1 = f_(__builtin_amdgcn_ds_bpermute(src, u_(x1))); gb = f_(__builtin_amdgcn_ds_bpermute(src, u_(gb)));
      PIN(x1); PIN(gb);
      STAMP(9);
      v4u c[6];
      const int co = (l >> 4) * 2560 + (l & 15) * 16;
      for (u32 spin = 0; spin < SPIN_CAP; ++spin) {
        CFENCE();
        u32 top = 0;
#pragma unroll
        for (int i = 0; i < 6; ++i) c[i] = ld4<SC1>(rws, co, WS_FC + i * 4 * 2560 + ru * 256);
#pragma unroll
        for (int i = 0; i < 6; ++i) top = umax(top, top4(c[i]));
        if (!any(top == SENT)) break;
      }
      STAMP(10);
      const v4f y = ((__builtin_bit_cast(v4f, c[0]) + __builtin_bit_cast(v4f, c[1])) + (__builtin_bit_cast(v4f, c[2]) + __builtin_bit_cast(v4f, c[3]))) +
                    (__builtin_bit_cast(v4f, c[4]) + __builtin_bit_cast(v4f, c[5]));
      const float ffn = sw16(sw32(y[0], y[2]), sw32(y[1], y[3]));
      const __bf16 res = (__bf16)(x1 + rb(gb * rb(ffn)));
      __builtin_amdgcn_raw_buffer_store_b16(__builtin_bit_cast(unsigned short, res), mk(P.out, 1280), src >> 1, ru * 128, 0);
      __builtin_amdgcn_raw_buffer_store_b64(v2u{tag, (u32)(tag64 >> 32)}, rws, (ru == 0 && l == 0) ? WS_E : OOB, 0, SC1);
      STAMP(11);
    }
  }
  STAMP(12);
  {
    const rsrc_t rd = mk((char*)P.ws + (1 << 20), 2048 * 16);
    const u32 val = ts[PROBE < 100 ? PROBE : 0] ? (u32)ts[PROBE < 100 ? PROBE : 0] : (u32)ts[0];
    st4(rd, l == 0 ? 0 : OOB, (b * 8 + w) * 16, v4u{(u32)ts[0], tag, val, tag});
    if (b == 0 && w == 0) {
      u32 mn = 0x7fffffff, mx = 0; u32 sum = 0;
      const u32 ref = (u32)ts[0] - 1000;
      for (int i = 0; i < 32; ++i) {
        v4u t;
        for (u32 spin = 0; spin < (1u << 20); ++spin) { CFENCE(); t = ld4<SC1>(rd, l * 16, i * 1024); if (!any((t.y != tag) | (t.w != tag))) break; }
        mn = min(mn, t.x - ref); mx = umax(mx, t.z - ref); sum += t.z - ref;
      }
      for (int o = 32; o > 0; o >>= 1) { mn = min(mn, (u32)__shfl_xor((int)mn, o)); mx = umax(mx, (u32)__shfl_xor((int)mx, o)); sum += (u32)__shfl_xor((int)sum, o); }
      u32 X = (PMODE == 0 ? mx : sum / 2048) - mn;
      if (PROBE == 100) X = ((u32)(u64)P.kc & 0xfff) / 16 + 1000 * (((u32)(u64)P.kc >> 12) & 0x1ff ? 1 : 0);
      if (PROBE == 101) X = ((u32)(u64)P.wq & 0xfff) / 16 + 1000 * (((u32)(u64)P.wq >> 12) & 0x1ff ? 1 : 0);
      X = __builtin_amdgcn_readfirstlane(X);
      const u32 until = __builtin_amdgcn_readfirstlane(ref + mn) + 10000 + 10 * X;
      while ((int)((u32)__builtin_amdgcn_s_memrealtime() - until) < 0) __builtin_amdgcn_s_sleep(1);
    }
  }
}
__global__ void code_end_marker() { asm volatile(".Lcode_end:"); }

extern "C" void kernel_launch(void* const* d_in, const int* in_sizes, int n_in, void* d_out, int out_size, void* d_ws,
                              size_t ws_size, hipStream_t stream) {
  Params P;
  P.x = d_in[0]; P.kc = d_in[1]; P.vc = d_in[2]; P.wq = d_in[3]; P.wo = d_in[4];
  P.wg = d_in[6]; P.wu = d_in[7]; P.wd = d_in[8]; P.c = d_in[9]; P.wmod = d_in[10];
  P.out = d_out; P.ws = d_ws;
  fused_block<<<256, 512, 0, stream>>>(P);
}
```

```cpp
#include <hip/hip_runtime.h>
#include <stdint.h>
#include <type_traits>

typedef unsigned u32;
typedef unsigned long long u64;
typedef u32 v4u __attribute__((ext_vector_type(4)));
typedef u32 v3u __attribute__((ext_vector_type(3)));
typedef u32 v2u __attribute__((ext_vector_type(2)));
typedef float v4f __attribute__((ext_vector_type(4)));
typedef float v2f __attribute__((ext_vector_type(2)));
typedef __bf16 bf16x2 __attribute__((ext_vector_type(2)));
typedef __bf16 bf16x8 __attribute__((ext_vector_type(8)));
typedef short v4s __attribute__((ext_vector_type(4)));
typedef __amdgpu_buffer_rsrc_t rsrc_t;
#define DEV __device__ __forceinline__
#define UNLIKELY(x) (!(ABL & 1) && __builtin_expect(!!(x), 0))
#define LAS __attribute__((address_space(3)))

#define KNOB(name, val) constexpr int name = val
#define T_ALL 1680
#define T_OFF 0
#define STAG 3
#define T_LEAD 0
#define T_SLOW 30
#define T_SLOW0 10
#define T_E 260
#define T_A 160
#define T_B 0
#define T_GP 0
#define T_DP 0
#define T_DT 0
#define T_CD 90
#define WK 3
#define WKD 5
#define WV 4
#define WVD 6
#define KFIRST 4
#define OEARLY 12
#define NMID 0
#define TPOS 20
#define GPOS 17
#define DTOUCH 1
#define QALT 0
#define HPOS 20
#define QPOS 1
#define VQK 6
#define STPOS 26
#define UNSTAG 28
#define CODEPF 1
#define ABL 0

constexpr int D = 640, DH = 192, SK = 32768, WIN = 4096;
constexpr int NT = 2, SC1 = 16;
constexpr u32 SENT = 0xFFFFFFFFu;
constexpr u32 SPIN_CAP = (ABL & 1) ? 1 : 1u << 16;
constexpr int WS_E = 0, WS_MOD = 1024, WS_Q = 16384, WS_ST = 40960, WS_OA = 45056, WS_AB = WS_OA + 98304, WS_FC = WS_AB + 20480,
              WS_END = WS_FC + 3072;
constexpr int L_CNT = 0, L_ST = 64, L_Q = 128, L_RED = 512, L_R2 = L_RED + 8 * 768, L_A = L_R2 + 2560, L_T = L_A + 4 * 960, L_TR = L_T + 8 * 6144, L_END = L_TR + 2560;

struct Params {
  const void* kc; const void* wmod; void* ws; const void* vc; const void* wq; const void* wo; const void* wg; const void* c;
  const void* x; const void* wu; const void* wd; void* out;
};
#define TV(i, c) (c)

DEV rsrc_t mk(const void* p, u32 bytes) { return __builtin_amdgcn_make_buffer_rsrc((void*)p, 0, bytes, 0x00020000); }
template <int AUX> DEV v4u ld4(rsrc_t r, int v, int s) { return __builtin_amdgcn_raw_buffer_load_b128(r, v, s, AUX); }
template <int AUX> DEV v3u ld3(rsrc_t r, int v, int s) { return __builtin_amdgcn_raw_buffer_load_b96(r, v, s, AUX); }
template <int AUX> DEV v2u ld2(rsrc_t r, int v, int s) { return __builtin_amdgcn_raw_buffer_load_b64(r, v, s, AUX); }
template <int AUX> DEV u32 ld1(rsrc_t r, int v, int s) { return __builtin_amdgcn_raw_buffer_load_b32(r, v, s, AUX); }
DEV void st4(rsrc_t r, int v, int s, v4u d) { __builtin_amdgcn_raw_buffer_store_b128(d, r, v, s, SC1); }
DEV float f_(u32 x) { return __builtin_bit_cast(float, x); }
DEV u32 u_(float x) { return __builtin_bit_cast(u32, x); }
DEV float lo_(u32 x) { return f_(x << 16); }
DEV float hi_(u32 x) { return f_(x & 0xffff0000u); }
DEV u32 pk(float a, float b) { bf16x2 t = {(__bf16)a, (__bf16)b}; return __builtin_bit_cast(u32, t); }
DEV float rb(float a) { return (float)(__bf16)a; }
DEV float rcp(float a) { return __builtin_amdgcn_rcpf(a); }
DEV u32 nz(u32 x) { return x < 0xFFFFFFFEu ? x : 0xFFFFFFFEu; }
DEV u32 umax(u32 a, u32 b) { return a > b ? a : b; }
DEV u32 top4(v4u x) { return umax(umax(x.x, x.y), umax(x.z, x.w)); }
DEV bool any(bool c) { return __builtin_amdgcn_ballot_w64(c) != 0; }
DEV float dot2(u32 a, u32 b, float c) {
  return __builtin_amdgcn_fdot2_f32_bf16(__builtin_bit_cast(bf16x2, a), __builtin_bit_cast(bf16x2, b), c, false);
}
template <int CTRL> DEV float dpp(float x) { return f_(__builtin_amdgcn_mov_dpp(u_(x), CTRL, 0xf, 0xf, true)); }
template <int CTRL> DEV u32 dppu(u32 x) { return __builtin_amdgcn_mov_dpp(x, CTRL, 0xf, 0xf, true); }
constexpr int X1 = 0xB1, X2 = 0x4E, X3 = 0x1B, HM = 0x141, R8 = 0x128;
template <int AND, int OR> DEV u32 swz(u32 x) { return __builtin_amdgcn_ds_swizzle(x, AND | (OR << 5)); }
DEV float sw32(float a, float b) {
  auto t = __builtin_amdgcn_permlane32_swap(u_(a), u_(b), false, false); return f_(t[0]) + f_(t[1]);
}
DEV float sw16(float a, float b) {
  auto t = __builtin_amdgcn_permlane16_swap(u_(a), u_(b), false, false); return f_(t[0]) + f_(t[1]);
}
DEV float mx32(float a) { auto t = __builtin_amdgcn_permlane32_swap(u_(a), u_(a), false, false); return fmaxf(f_(t[0]), f_(t[1])); }
DEV float mx16(float a) { auto t = __builtin_amdgcn_permlane16_swap(u_(a), u_(a), false, false); return fmaxf(f_(t[0]), f_(t[1])); }
DEV float sum8(float a) { a += dpp<X1>(a); a += dpp<X2>(a); a += dpp<HM>(a); return a; }
DEV float sumhi(float a) { a += dpp<R8>(a); a = sw16(a, a); return sw32(a, a); }
DEV float max8(float a) { a = fmaxf(a, dpp<X1>(a)); a = fmaxf(a, dpp<X2>(a)); return fmaxf(a, dpp<HM>(a)); }
DEV float maxhi(float a) { a = fmaxf(a, dpp<R8>(a)); return mx32(mx16(a)); }
DEV float sum64(float a) { return sumhi(sum8(a)); }
DEV float ex(float a) { return __builtin_amdgcn_exp2f(a * 1.4426950408889634f); }
DEV v4f mfma(v4u a, v4u b, v4f c) {
  return __builtin_amdgcn_mfma_f32_16x16x32_bf16(__builtin_bit_cast(bf16x8, a), __builtin_bit_cast(bf16x8, b), c, 0, 0, 0);
}
DEV v4u tr_frag(const char* p, int half) {
  const v2u a = __builtin_bit_cast(v2u, __builtin_amdgcn_ds_read_tr16_b64_v4i16((LAS v4s*)p));
  const v2u b = __builtin_bit_cast(v2u, __builtin_amdgcn_ds_read_tr16_b64_v4i16((LAS v4s*)(p + half)));
  v4u r = {a.x, a.y, b.x, b.y};
  return r;
}
DEV u32 plo(u32 a, u32 b) { return __builtin_amdgcn_perm(b, a, 0x05040100u); }
DEV u32 phi(u32 a, u32 b) { return __builtin_amdgcn_perm(b, a, 0x07060302u); }
#define SB() __builtin_amdgcn_sched_barrier(0)
#define PACE(n) do { asm volatile("s_waitcnt vmcnt(%0)" :: "n"((n) - 1) : "memory"); SB(); } while (0)
#define GATE(dl) do { \
    asm volatile("s_branch 2f\n1:\n\ts_sleep 1\n2:\n\ts_memrealtime s[96:97]\n\ts_waitcnt lgkmcnt(0)\n\ts_lshl_b32 s96, s96, 6\n\ts_sub_u32 s96, s96, %0\n\ts_cmp_lt_i32 s96, 0\n\ts_cbranch_scc1 1b" \
                 :: "s"(dl) : "memory", "scc", "s96", "s97"); SB(); } while (0)
#define TR(pos)
#define LANDED(x) do { asm volatile("" :: "v"(x)); SB(); } while (0)
#define SLOT(n, deep) do { \
    asm volatile("s_waitcnt vmcnt(%3)\n\ts_branch 2f\n1:\n\ts_sleep 1\n2:\n\ts_memrealtime s[96:97]\n\ts_waitcnt lgkmcnt(0)\n\ts_lshl_b32 s96, s96, 6\n\t" \
                 "s_sub_u32 s97, s96, %0\n\ts_cmp_lt_i32 s97, 0\n\ts_cbranch_scc1 1b\n\ts_cmp_ge_i32 s97, %1\n\ts_cbranch_scc1 3f\n\ts_waitcnt vmcnt(%2)\n3:" \
                 :: "s"(dl), "s"(late), "n"(n), "n"(deep) : "memory", "scc", "s96", "s97"); SB(); } while (0)
#define BARE() do { asm volatile("s_barrier" ::: "memory"); SB(); } while (0)
#define LWAIT() asm volatile("s_waitcnt lgkmcnt(0)" ::: "memory")
#define BAR() do { asm volatile("s_waitcnt lgkmcnt(0)\n\ts_barrier" ::: "memory"); SB(); } while (0)
#define CFENCE() asm volatile("" ::: "memory")
#define PIN(x) asm volatile("" : "+v"(x))
constexpr int OOB = 0x7ffffff0;

#define STAMP(i)
#define TSARG
#define TSPASS

DEV void pair_rows(const v4u a, const v4u b, v4u& p0, v4u& p1) {
  p0 = v4u{plo(a.x, b.x), phi(a.x, b.x), plo(a.y, b.y), phi(a.y, b.y)};
  p1 = v4u{plo(a.z, b.z), phi(a.z, b.z), plo(a.w, b.w), phi(a.w, b.w)};
  PIN(p0); PIN(p1);
}
template <int J> DEV u32 bc8(u32 a) { return swz<0x18, J>(a); }
DEV void spread(u32 a, u32 (&s)[6]) { s[0] = bc8<0>(a); s[1] = bc8<1>(a); s[2] = bc8<2>(a); s[3] = bc8<3>(a); s[4] = bc8<4>(a); s[5] = bc8<5>(a); }
template <int N> DEV float tile_dot(const v4u (&W)[12], const u32 (&s)[6], int l) {
  float a[8];
#pragma unroll
  for (int c = 0; c < 8; ++c) a[c] = 0.f;
#pragma unroll
  for (int j = 0; j < N; ++j)
#pragma unroll
    for (int c = 0; c < 8; ++c) a[c] = dot2(W[2 * j + (c >> 2)][c & 3], s[j], a[c]);
  const float y0 = sw32(a[0], a[4]), y1 = sw32(a[1], a[5]), y2 = sw32(a[2], a[6]), y3 = sw32(a[3], a[7]);
  float z0 = sw16(y0, y2), z1 = sw16(y1, y3);
  z0 += dpp<R8>(z0); z1 += dpp<R8>(z1);
  return (l & 8) ? z1 : z0;
}
DEV int tile_col(int l) { return (l & 7) * 8 + (l >> 5) * 4 + ((l >> 4) & 1) * 2 + ((l >> 3) & 1); }
DEV float col_sum(const char* p) {
  const float* f = (const float*)p;
  return ((f[0] + f[64]) + (f[128] + f[192])) + ((f[256] + f[320]) + (f[384] + f[448]));
}
DEV float silu_b(float v) { return v / (1.f + ex(-v)); }

template <int I, int N, class F> DEV void sfor(F&& f) {
  if constexpr (I < N) { f(std::integral_constant<int, I>{}); sfor<I + 1, N>(f); }
}
enum { RQ = 0, RO = 1, RE = 2, RM = 3 };
constexpr int extras_at(int role, int n1, int p) {
  return (p == KFIRST ? 1 : 0) + (p == KFIRST && role == RQ ? 3 : 0) + (p == KFIRST && role == RM ? 1 : 0) + (p == HPOS && role == RQ ? 2 : 0) +
         (p == n1 + QPOS ? 1 : 0) + (p == n1 + GPOS && role == RQ ? 4 : 0) + (p == n1 + STPOS ? 1 : 0) + (p == n1 + TPOS && NMID > 0 ? 2 : 0);
}
constexpr int pending(int role, int n1, int p, int win) {
  int n = win - 1;
  for (int q = p - win + 1; q <= p; ++q) n += extras_at(role, n1, q);
  return n;
}

template <int ROLE>
DEV void run(const Params& P, char* lds, const rsrc_t rk, const size_t kvbase, const int kvo, v4u (&K)[24], const u32 cpf, const u32 ecall, const u32 t0,
             const int b, const int w, const int l TSARG) {
  const int xg = b & 7, idx = b >> 3, head = 4 * xg + (idx >> 3), sp = idx & 7;
  const int r8 = l >> 3, c8 = l & 7, cp = l & 15, kq = l >> 4;
  constexpr int stride = ROLE == RQ ? 12288 : ROLE == RO ? 1280 : 7680;
  constexpr int nrow = ROLE == RO ? 12 : 10;
  constexpr int NW = ROLE == RO ? OEARLY : 10;
  constexpr int N1 = 24 + NW;
  constexpr int J0 = KFIRST;
  const int unit = ROLE == RQ ? 12 * xg + idx : ROLE == RO ? idx - 12 : b - 176;
  const bool notile = ROLE == RM && unit >= 60;
  const int row0 = (ROLE == RO ? 768 * xg : 0) + w * 8 * nrow;
  const char* wbase = (const char*)(ROLE == RQ ? P.wq : ROLE == RO ? P.wo : P.wmod);
  const rsrc_t rw = mk(wbase + row0 * stride + unit * 128, notile ? 0 : 8 * nrow * stride - unit * 128);
  const int wvo = r8 * (ROLE == RO ? 1 : 10) * stride + c8 * 16;
  constexpr int wstep = (ROLE == RO ? 8 : 1) * stride;
  char* stage = lds + L_T + w * 6144;
  const rsrc_t rws = mk(P.ws, (ABL & 2) ? 0 : WS_END);
  v4u W[12];
  const int pe = w * 80 + r8 * 10 + (c8 < 5 ? c8 : 4) * 2;
  u32 ap = 0;
  v4u xa; u32 xb;
  if constexpr (ROLE == RE) {
#pragma unroll
    for (int i = 0; i < KFIRST; ++i) W[i] = K[i];
  }
  if constexpr (ROLE != RO) ap = ld1<0>(mk(ROLE == RQ ? P.x : P.c, 1280), pe * 2, 0);
  if constexpr (ROLE == RQ) { const rsrc_t r = mk(P.x, w == 0 ? 1280 : 0); xa = ld4<0>(r, l * 20, 0); xb = ld1<0>(r, l * 20 + 16, 0); }
  SB();
  STAMP(13);
  constexpr bool ALT = ROLE == RO || ROLE == RM || (ROLE == RQ && QALT);
  constexpr int LASTW = ALT ? J0 + 2 * (NW - 1) : ROLE == RE ? NW - 1 : J0 + NW - 1;
  constexpr int USEW = ROLE == RQ ? HPOS + WKD : ROLE == RE ? LASTW + 2 : LASTW + WKD;
  constexpr int LINES = 8 * (ROLE == RE ? N1 - USEW - 1 : N1) + 192 + NMID;
  const int tbeg = ROLE == RE ? TV(12, T_E) : TV(1, T_OFF), tlen = TV(1, T_OFF) + TV(0, T_ALL) - tbeg;
  const u32 step = notile ? (u32)(tlen * 64) / (LINES - 80) : (u32)(tlen * 64) / LINES;
  const u32 stepw = notile ? 0 : 8 * step;
  const u32 tend = (t0 + TV(1, T_OFF) + TV(0, T_ALL)) << 6;
  u32 dl = ((t0 + tbeg + w * TV(2, STAG)) << 6) + (ROLE == RE ? 0 : 8 * J0 * step);
  u32 late = TV(11, T_SLOW0) << 6;
  const u32 lstep = TV(10, T_SLOW) - TV(11, T_SLOW0);
  u32 tag = 0;

  auto wo_frags = [&](int ks) {
#pragma unroll
    for (int i = 0; i < 4; ++i) *(v4u*)(stage + i * 1024 + l * 16) = W[4 * ks + i];
    LWAIT();
#pragma unroll
    for (int t = 0; t < 4; ++t) W[4 * ks + t] = tr_frag(stage + (8 * kq + (cp >> 2)) * 128 + (cp & 3) * 8 + t * 32, 512);
    PIN(W[4 * ks]); PIN(W[4 * ks + 1]); PIN(W[4 * ks + 2]); PIN(W[4 * ks + 3]);
    CFENCE();
  };
  auto wsoff = [](int i) { return ROLE == RO ? (3 * (i & 3) + (i >> 2)) * wstep : i * wstep; };
  v2u gs, gc;
  float ln0, ln1;
  u32 sc[6];
  sfor<J0, N1>([&](auto jc) {
    constexpr int j = jc;
    constexpr bool isw = j <= LASTW && (!ALT || ((j - J0) & 1) == 0);
    constexpr int wi = ALT ? (j - J0) / 2 : ROLE == RE ? j : j - J0;
    constexpr int ki = j <= LASTW ? J0 + (j - J0) / 2 : j - NW;
    if constexpr (ROLE == RQ && j == HPOS) { gs = ld2<SC1>(rws, pe * 4, WS_MOD); gc = ld2<SC1>(rws, pe * 4, WS_MOD + 2560); SB(); }
    if constexpr (ROLE != RE || j > USEW) {
      SLOT(pending(ROLE, N1, j, WK), pending(ROLE, N1, j, WKD));
      dl += isw ? stepw : 8 * step;
    }
    late += lstep;
    TR(j);
    if constexpr (isw) W[wi] = ld4<NT>(rw, wvo, wsoff(wi));
    else K[ki] = ld4<NT>(rk, kvo + (ki % 3) * 128, (ki / 3) * 3072);
    SB();
    if constexpr (j == 9) {
      tag = __builtin_amdgcn_readfirstlane(ecall) * 0x4C957F2Du + 0xF767814Fu;
      asm volatile("" :: "v"(cpf));
      if constexpr (ROLE == RE || ROLE == RM) {
        spread(pk(silu_b(lo_(ap)), silu_b(hi_(ap))), sc);
        PIN(sc[0]); PIN(sc[1]); PIN(sc[2]); PIN(sc[3]); PIN(sc[4]);
      }
      if constexpr (ROLE == RQ) {
        if (w == 0) {
          float v[10] = {lo_(xa.x), hi_(xa.x), lo_(xa.y), hi_(xa.y), lo_(xa.z), hi_(xa.z), lo_(xa.w), hi_(xa.w), lo_(xb), hi_(xb)};
          float s = 0.f, q = 0.f;
#pragma unroll
          for (int i = 0; i < 10; ++i) s += v[i];
          const float mean = sum64(s) * (1.f / D);
#pragma unroll
          for (int i = 0; i < 10; ++i) { const float d = v[i] - mean; q = fmaf(d, d, q); }
          const float var = sum64(q) * (1.f / D);
          *(v2f*)(lds + L_CNT) = v2f{rb(mean), rcp(rb(__builtin_amdgcn_sqrtf(rb(rb(var) + 1e-5f))))};
        }
      }
      SB();
    }
    if constexpr (ROLE == RQ && j == 12) {
      BAR();
      const v2f t = *(const v2f*)(lds + L_CNT);
      ln0 = rb(rb(lo_(ap) - t[0]) * t[1]); ln1 = rb(rb(hi_(ap) - t[0]) * t[1]);
      PIN(ln0); PIN(ln1);
      SB();
    }
    if constexpr (ROLE != RO && j == (ROLE == RE ? USEW : USEW - 1)) {
#pragma unroll
      for (int i = 0; i < 5; ++i) pair_rows(W[2 * i], W[2 * i + 1], W[2 * i], W[2 * i + 1]);
      SB();
    }
    if constexpr (ROLE == RO && j >= USEW && j < USEW + OEARLY / 4) { wo_frags(j - USEW); SB(); }
    if constexpr (ROLE != RO && j == USEW) {
      STAMP(1);
      if constexpr (ROLE == RQ) {
        if (UNLIKELY(any((gs.y != tag) | (gc.y != tag)))) {
          for (u32 spin = 0; spin < SPIN_CAP; ++spin) {
            CFENCE();
            gs = ld2<SC1>(rws, pe * 4, WS_MOD); gc = ld2<SC1>(rws, pe * 4, WS_MOD + 2560);
            if (!any((gs.y != tag) | (gc.y != tag))) break;
          }
        }
        spread(pk(rb(ln0 * rb(1.f + lo_(gc.x))) + lo_(gs.x), rb(ln1 * rb(1.f + hi_(gc.x))) + hi_(gs.x)), sc);
      }
      *(float*)(lds + L_R2 + w * 256 + tile_col(l) * 4) = tile_dot<5>(W, sc, l);
      SB();
    }
    if constexpr (ROLE != RO && j == (ROLE == RE ? USEW : USEW + 1)) {
      BAR();
      if (w == 0) {
        const float v = col_sum(lds + L_R2 + l * 4);
        const u32 p0 = pk(v, dpp<X1>(v)), p1 = dppu<X2>(p0);
        st4(rws, ((l & 3) == 0 && !notile) ? l * 4 : OOB, (ROLE == RQ ? WS_Q : WS_MOD) + unit * 256, v4u{p0, tag, p1, tag});
      }
      SB();
      STAMP(2);
    }
  });
  const rsrc_t rv = mk((const char*)P.vc + kvbase, 64 * 384);
  const int vvo = (l >> 5) * 4 * 384 + (l & 31) * 12;
  const int qo = (l < 48 ? l : 0) * 16;
  v3u V[32];
  v4u qg, st;
  v2u touch[4] = {{0, 0}, {0, 0}, {0, 0}, {0, 0}};
  auto send_ffn = [&](int part) {
    const int cnt = part ? 15 - NMID : NMID;
    const int n = (part ? 128 * NMID : 0) + (idx * 4 + (w >> 1)) * cnt + l, r = (n * 21846) >> 16;
    int o = (160 * ((r >> 5) & 3) + 32 * (r >> 7) + (r & 31)) * 3072 + (n - 3 * r) * 128 + (l < cnt ? 0 : 0x40000000);
    PIN(o);
    const rsrc_t rr = mk((w & 1) ? P.wu : P.wg, 640 * 3072);
    if (part) BARE();
    touch[2 * part] = ld2<NT>(rr, o, 384 * xg); touch[2 * part + 1] = ld2<SC1>(rr, o, 384 * xg + 64);
    SB();
  };
  const int fo = (w < 4 && l < 40) ? (160 * w + 4 * l) * 4 : OOB;
  v4u gg, gb2, gc2; v2u gx;
  auto vload = [&](auto ic) {
    constexpr int i = ic;
    if constexpr (ROLE == RQ && i == GPOS) {
      gg = ld4<SC1>(rws, fo, WS_MOD + 2 * 2560); gb2 = ld4<SC1>(rws, fo, WS_MOD + 3 * 2560); gc2 = ld4<SC1>(rws, fo, WS_MOD + 4 * 2560);
      gx = ld2<0>(mk(P.x, 1280), fo >> 1, 0);
      SB();
    }
    if constexpr (i == QPOS) { qg = ld4<SC1>(mk(P.ws, (w == 0 && !(ABL & 2)) ? WS_END : 0), qo, WS_Q + head * 768); SB(); }
    if constexpr (i == STPOS) { st = ld4<SC1>(rws, c8 * 16, WS_ST + head * 128); SB(); }
    if constexpr (i == TPOS && NMID > 0) { GATE(dl); dl += NMID * step; send_ffn(0); }
    if constexpr (i == UNSTAG) dl -= (w * TV(2, STAG) + TV(3, T_LEAD)) << 6;
    SLOT(pending(ROLE, N1, N1 + i, WV), pending(ROLE, N1, N1 + i, WVD));
    dl += 6 * step; late += lstep;
    TR(40 + i);
    V[i] = ld3<NT>(rv, vvo + (i & 3) * 384, (i >> 2) * 3072);
    SB();
  };
  sfor<0, VQK>(vload);
  STAMP(4);
  if (w == 0) {
    if (UNLIKELY(any((qg.y != tag) | (qg.w != tag)))) {
      for (u32 spin = 0; spin < SPIN_CAP; ++spin) {
        CFENCE();
        qg = ld4<SC1>(rws, qo, WS_Q + head * 768);
        if (!any((qg.y != tag) | (qg.w != tag))) break;
      }
    }
    *(v2u*)(lds + L_Q + (qo >> 1)) = v2u{qg.x, qg.z};
  }
  BAR();
  STAMP(5);
  float p[8], mw;
  {
    v4u qf[3];
#pragma unroll
    for (int k = 0; k < 3; ++k) qf[k] = *(const v4u*)(lds + L_Q + k * 128 + c8 * 16);
    sfor<0, 8>([&](auto tc) {
      constexpr int t = tc;
      float s0 = 0.f, s1 = 0.f;
#pragma unroll
      for (int k = 0; k < 3; ++k) {
        s0 = dot2(K[3 * t + k][0], qf[k][0], s0); s1 = dot2(K[3 * t + k][1], qf[k][1], s1);
        s0 = dot2(K[3 * t + k][2], qf[k][2], s0); s1 = dot2(K[3 * t + k][3], qf[k][3], s1);
      }
      p[t] = rb(rb(sum8(s0 + s1)) * 0.07216878364870322f);
      PIN(p[t]);
      SB();
      vload(std::integral_constant<int, VQK + t>{});
    });
    mw = maxhi(fmaxf(fmaxf(fmaxf(p[0], p[1]), fmaxf(p[2], p[3])), fmaxf(fmaxf(p[4], p[5]), fmaxf(p[6], p[7]))));
    PIN(mw);
    SB();
    vload(std::integral_constant<int, VQK + 8>{});
    float ls = 0.f;
#pragma unroll
    for (int t = 0; t < 8; ++t) { p[t] = ex(p[t] - mw); ls += p[t]; }
    ls = sumhi(ls);
    *(v2f*)(lds + L_ST + w * 8) = v2f{mw, ls};
    SB();
    vload(std::integral_constant<int, VQK + 9>{});
    BAR();
    if (w == 0) {
      const v2f t = *(const v2f*)(lds + L_ST + c8 * 8);
      const float mc = max8(t[0]), lc = sum8(t[1] * ex(t[0] - mc));
      st4(rws, l == 0 ? 0 : OOB, WS_ST + (head * 8 + sp) * 16, v4u{u_(mc), tag, u_(lc), tag});
    }
    SB();
  }
  STAMP(14);
  v2f acc[3] = {{0.f, 0.f}, {0.f, 0.f}, {0.f, 0.f}};
  float pb[32];
  auto pspread = [&](int t) {
    pb[4 * t] = f_(swz<0, 0>(u_(p[t]))); pb[4 * t + 1] = f_(swz<0, 8>(u_(p[t])));
    pb[4 * t + 2] = f_(swz<0, 16>(u_(p[t]))); pb[4 * t + 3] = f_(swz<0, 24>(u_(p[t])));
    if (t == 7) { PIN(pb[28]); PIN(pb[29]); PIN(pb[30]); PIN(pb[31]); }
  };
  auto pv = [&](int i) {
    if ((i & 3) == 0 && i < 28) pspread(i >> 2);
    const v2f pp = {pb[i], pb[i]};
#pragma unroll
    for (int d = 0; d < 3; ++d) acc[d] = __builtin_elementwise_fma(pp, v2f{lo_(V[i][d]), hi_(V[i][d])}, acc[d]);
    asm volatile("" : "+v"(acc[0]), "+v"(acc[1]), "+v"(acc[2]));
    SB();
  };
  constexpr int STCHK = 28;
  constexpr int LAG = WVD, FIRST = VQK + 10;
  sfor<FIRST, 32>([&](auto ic) {
    constexpr int i = ic;
    vload(ic);
    constexpr int lo = 2 * (i - FIRST) < i - LAG ? 2 * (i - FIRST) : i - LAG, hi = 2 * (i + 1 - FIRST) < i + 1 - LAG ? 2 * (i + 1 - FIRST) : i + 1 - LAG;
    if constexpr (i < 31) {
#pragma unroll
      for (int t = lo; t < hi; ++t) pv(t);
    }
  });
  constexpr int done = 31 - LAG;
  static_assert(2 * (31 - FIRST) >= done && done <= STCHK, "backlog");
  if constexpr (NMID < 15) send_ffn(1); else if constexpr (ROLE == RO && OEARLY < 12) BARE();
  if constexpr (ROLE == RO) {
#pragma unroll
    for (int i = OEARLY; i < 12; ++i) W[i] = ld4<NT>(rw, wvo, wsoff(i));
  }
  SB();
  STAMP(6);
  if (UNLIKELY(any((st.y != tag) | (st.w != tag)))) {
    for (u32 spin = 0; spin < SPIN_CAP; ++spin) {
      CFENCE();
      st = ld4<SC1>(rws, c8 * 16, WS_ST + head * 128);
      if (!any((st.y != tag) | (st.w != tag))) break;
    }
  }
  const float M = max8(f_(st.x));
  const float scale = ex(mw - M) * rcp(sum8(f_(st.z) * ex(f_(st.x) - M)));
  p[7] *= scale;
  pspread(7);
  SB();
#pragma unroll
  for (int t = done; t < STCHK; ++t) pv(t);
#pragma unroll
  for (int d = 0; d < 3; ++d) acc[d] *= scale;
#pragma unroll
  for (int t = STCHK; t < 32; ++t) pv(t);
  STAMP(7);
  {
    float* dst = (float*)(lds + L_RED + w * 768 + (l & 31) * 24 + (l >> 5) * 12);
    dst[0] = sw32(acc[0][0], acc[1][1]); dst[1] = sw32(acc[0][1], acc[2][0]); dst[2] = sw32(acc[1][0], acc[2][1]);
  }
  BAR();
  if (w == 7) {
    const char* src = lds + L_RED + qo;
    v4f a = *(const v4f*)src;
#pragma unroll
    for (int i = 1; i < 8; ++i) a += *(const v4f*)(src + i * 768);
    const u32 d0 = nz(pk(a[0], a[1])), d1 = nz(pk(a[2], a[3]));
    const u32 e0 = dppu<X1>(d0), e1 = dppu<X1>(d1);
    st4(rws, (l < 48 && !(l & 1)) ? l * 8 : OOB, WS_OA + (head * 8 + sp) * 384, v4u{d0, d1, e0, e1});
  }
  SB();
  STAMP(8);
  if constexpr (ROLE == RO) {
#pragma unroll
    for (int ks = OEARLY / 4; ks < 3; ++ks) wo_frags(ks);
    SB();
    GATE(tend + (TV(4, T_A) << 6));
    STAMP(9);
    v4u a, a2;
    const int ao = (cp >> 1) * 384 + kq * 48, as = WS_OA + (4 * xg + (w >> 1)) * 3072 + (w & 1) * 192;
    for (u32 spin = 0; spin < SPIN_CAP; ++spin) {
      CFENCE();
      a = ld4<SC1>(rws, ao + (cp & 1) * 16, as); a2 = ld4<SC1>(rws, ao + 32, as);
      if (!any(umax(top4(a), top4(a2)) == SENT)) break;
    }
    STAMP(10);
    float e[4];
#pragma unroll
    for (int t = 0; t < 4; ++t) {
      const v4f z = {0.f, 0.f, 0.f, 0.f};
      const v4f d0 = mfma(a2, W[8 + t], mfma(a, W[t], z)), d1 = mfma(a, W[4 + t], z);
      e[t] = (d0[0] + d0[2]) + (d1[1] + d1[3]);
    }
    *(float*)(lds + L_R2 + w * 256 + l * 4) = sw16(sw32(e[0], e[2]), sw32(e[1], e[3]));
    BAR();
    if (w == 0) {
      const u32 v = nz(u_(col_sum(lds + L_R2 + l * 4)));
      st4(rws, (l & 3) == 0 ? l * 4 : OOB, WS_AB + xg * 2560 + unit * 256, v4u{v, dppu<X1>(v), dppu<X2>(v), dppu<X3>(v)});
    }
    STAMP(11);
  } else if constexpr (ROLE == RQ) {
    BAR();
    GATE(tend + (TV(7, T_GP) << 6));
    {
      const rsrc_t r = mk(w < 4 ? P.wg : P.wu, 640 * 3072);
#pragma unroll
      for (int i = 0; i < 5; ++i) W[i] = ld4<0>(r, (l >> 1) * 3072 + (l & 1) * 16, (160 * (w & 3) + 32 * i) * 3072 + unit * 32);
      SB();
    }
    if (UNLIKELY(any((fo != OOB) & ((gg.y != tag) | (gg.w != tag) | (gb2.y != tag) | (gb2.w != tag) | (gc2.y != tag) | (gc2.w != tag))))) {
      for (u32 spin = 0; spin < SPIN_CAP; ++spin) {
        CFENCE();
        gg = ld4<SC1>(rws, fo, WS_MOD + 2 * 2560); gb2 = ld4<SC1>(rws, fo, WS_MOD + 3 * 2560); gc2 = ld4<SC1>(rws, fo, WS_MOD + 4 * 2560);
        if (!any((fo != OOB) & ((gg.y != tag) | (gg.w != tag) | (gb2.y != tag) | (gb2.w != tag) | (gc2.y != tag) | (gc2.w != tag)))) break;
      }
    }
    v2f ga0 = {lo_(gg.x), hi_(gg.x)}, ga1 = {lo_(gg.z), hi_(gg.z)}, xv0 = {lo_(gx.x), hi_(gx.x)}, xv1 = {lo_(gx.y), hi_(gx.y)};
    const u32 c01 = pk(1.f + lo_(gc2.x), 1.f + hi_(gc2.x)), c23 = pk(1.f + lo_(gc2.z), 1.f + hi_(gc2.z));
    v2f cb0 = {lo_(c01), hi_(c01)}, cb1 = {lo_(c23), hi_(c23)};
    char* arow = lds + L_A + (w & 3) * 960;
    if (fo != OOB) { *(v2u*)(arow + 320 + l * 8) = v2u{c01, c23}; *(v2u*)(arow + 640 + l * 8) = v2u{gb2.x, gb2.z}; }
    PIN(ga0); PIN(ga1); PIN(xv0); PIN(xv1); PIN(cb0); PIN(cb1);
    SB();
#pragma unroll
    for (int i = 0; i < 5; ++i) *(v4u*)(stage + i * 1024 + l * 16) = W[i];
    BAR();
    if (w < 4) {
#pragma unroll
      for (int i = 0; i < 5; ++i) {
        W[i] = tr_frag(stage + i * 1024 + kq * 256 + cp * 8, 128); W[5 + i] = tr_frag(stage + 4 * 6144 + i * 1024 + kq * 256 + cp * 8, 128);
        PIN(W[i]); PIN(W[5 + i]);
      }
      SB();
      GATE(tend + (TV(5, T_B) << 6));
      STAMP(9);
      v4u a[8];
      v2f x01, x23; float s1;
      for (u32 spin = 0; spin < SPIN_CAP; ++spin) {
        CFENCE();
#pragma unroll
        for (int g = 0; g < 8; ++g) a[g] = ld4<SC1>(rws, fo, WS_AB + g * 2560);
        const v4f sm = ((__builtin_bit_cast(v4f, a[0]) + __builtin_bit_cast(v4f, a[1])) + (__builtin_bit_cast(v4f, a[2]) + __builtin_bit_cast(v4f, a[3]))) +
                       ((__builtin_bit_cast(v4f, a[4]) + __builtin_bit_cast(v4f, a[5])) + (__builtin_bit_cast(v4f, a[6]) + __builtin_bit_cast(v4f, a[7])));
        x01 = __builtin_elementwise_fma(ga0, v2f{sm[0], sm[1]}, xv0); x23 = __builtin_elementwise_fma(ga1, v2f{sm[2], sm[3]}, xv1);
        const v2f t = x01 + x23;
        s1 = t[0] + t[1];
        if (!any(s1 != s1)) break;
        u32 top = 0;
#pragma unroll
        for (int g = 0; g < 8; ++g) top = umax(top, top4(a[g]));
        if (!any(top == SENT)) break;
      }
      STAMP(10);
      const v2f y0 = x01 * cb0, y1 = x23 * cb1;
      if (l < 40) *(v2u*)(arow + l * 8) = v2u{pk(y0[0], y0[1]), pk(y1[0], y1[1])};
      const v2f q2 = __builtin_elementwise_fma(x01, x01, x23 * x23);
      float t = sw32(s1, q2[0] + q2[1]);
      LWAIT();
      v4f dg = {0.f, 0.f, 0.f, 0.f}, du = {0.f, 0.f, 0.f, 0.f};
      const char* ard = arow + ((cp & 3) < 3 ? (cp & 3) : 2) * 320 + kq * 16;
#pragma unroll
      for (int i = 0; i < 5; ++i) {
        const v4u af = *(const v4u*)(ard + i * 64);
        dg = mfma(af, W[i], dg); du = mfma(af, W[5 + i], du);
      }
      t = sum8(t); t += dpp<R8>(t); t = sw16(t, t);
      *(float*)(lds + L_ST + (l >> 5) * 16 + w * 4) = t;
      *(v4f*)(lds + L_R2 + w * 256 + cp * 16) = dg;
      *(v4f*)(lds + L_R2 + 1024 + w * 256 + cp * 16) = du;
    }
    BAR();
    if (w == 0) {
      const v4f ss = *(const v4f*)(lds + L_ST), qq = *(const v4f*)(lds + L_ST + 16);
      const v4f dg = *(const v4f*)(lds + L_R2 + l * 16), du = *(const v4f*)(lds + L_R2 + 1024 + l * 16);
      const float mean = ((ss[0] + ss[1]) + (ss[2] + ss[3])) * (1.f / D);
      const float var = fmaxf(fmaf(-mean, mean, ((qq[0] + qq[1]) + (qq[2] + qq[3])) * (1.f / D)), 0.f);
      const float rs = __builtin_amdgcn_rsqf(var + 1e-5f);
      float t = sw32(fmaf(rs, fmaf(-mean, dg[1], dg[0]), dg[2]), fmaf(rs, fmaf(-mean, du[1], du[0]), du[2]));
      t = rb(sw16(t, t));
      const auto gu = __builtin_amdgcn_permlane32_swap(u_(t), u_(t), false, false);
      const float gt = f_(gu[0]), up = f_(gu[1]);
      const float u2 = gt * fmaf(gt * gt, -0.044715f * 2.f * 0.7978845608028654f * 1.4426950408889634f, -2.f * 0.7978845608028654f * 1.4426950408889634f);
      const float g = rb(gt * rcp(1.f + __builtin_amdgcn_exp2f(u2))) * up;
      const u32 gp = nz(pk(g, dpp<X1>(g)));
      st4(rws, (l & 55) == 0 ? l * 2 : OOB, WS_FC + unit * 32, v4u{gp, dppu<0x102>(gp), dppu<0x104>(gp), dppu<0x106>(gp)});
    }
    STAMP(11);
  } else {
    BAR();
    GATE(tend + (TV(13, T_DT) << 6));
    {
      const int n = ((idx - 22) * 8 + w) * 39 + l;
      const int o = l < 39 ? (n >> 1) * 1280 + (n & 1) * 128 : OOB;
      const u32 t2 = DTOUCH == 2 ? ld1<NT>(mk(P.wd, 1536 * 1280), o, (160 * xg) & ~127) : 0, t3 = ld1<SC1>(mk(P.wd, 1536 * 1280), o, ((160 * xg) & ~127) + 64);
      asm volatile("" :: "v"(t2), "v"(t3));
    }
    if (ROLE == RM && idx >= 27) {
      const int dch = 80 * xg + 16 * (idx - 27);
      const int mo = w == 0 ? (dch + (cp & 14)) * 4 : OOB;
      v2u da = ld2<SC1>(rws, mo, WS_MOD + 2 * 2560), db = ld2<SC1>(rws, mo, WS_MOD + 5 * 2560);
      const v2u dx = ld2<0>(mk(P.x, 1280), mo >> 1, 0);
      GATE(tend + (TV(8, T_DP) << 6));
      const rsrc_t r = mk(P.wd, 1536 * 1280);
#pragma unroll
      for (int i = 0; i < 6; ++i) W[i] = ld4<0>(r, (l >> 1) * 1280 + (l & 1) * 16, (192 * w + 32 * i) * 1280 + dch * 2);
      SB();
#pragma unroll
      for (int i = 0; i < 6; ++i) *(v4u*)(stage + i * 1024 + l * 16) = W[i];
      LWAIT();
#pragma unroll
      for (int i = 0; i < 6; ++i) { W[i] = tr_frag(stage + (6 * kq + i) * 256 + cp * 8, 128); PIN(W[i]); }
      float x1 = 0.f, gb = 0.f;
      if (w == 0) {
        if (UNLIKELY(any((da.y != tag) | (db.y != tag)))) {
          for (u32 spin = 0; spin < SPIN_CAP; ++spin) {
            CFENCE();
            da = ld2<SC1>(rws, (dch + (cp & 14)) * 4, WS_MOD + 2 * 2560); db = ld2<SC1>(rws, (dch + (cp & 14)) * 4, WS_MOD + 5 * 2560);
            if (!any((da.y != tag) | (db.y != tag))) break;
          }
        }
        u32 a0, a1, spin;
        GATE(tend + (TV(5, T_B) << 6));
        for (spin = 0; spin < SPIN_CAP; ++spin) {
          CFENCE();
          a0 = ld1<SC1>(rws, kq * 2560 + cp * 4, WS_AB + dch * 4); a1 = ld1<SC1>(rws, kq * 2560 + cp * 4, WS_AB + 4 * 2560 + dch * 4);
          if (!any(umax(a0, a1) == SENT)) break;
        }
        const u32 tseen = (u32)__builtin_amdgcn_s_memrealtime();
        float sm = f_(a0) + f_(a1);
        sm = sw16(sm, sm); sm = sw32(sm, sm);
        x1 = rb(((l & 1) ? hi_(dx.x) : lo_(dx.x)) + rb(((l & 1) ? hi_(da.x) : lo_(da.x)) * rb(sm)));
        gb = (l & 1) ? hi_(db.x) : lo_(db.x);
        PIN(x1); PIN(gb);
        STAMP(3);
        GATE((tseen + (spin > 0 ? TV(9, T_CD) : TV(6, T_CD))) << 6);
      }
      BAR();
      STAMP(9);
      const int m3 = (cp & 3) < 3 ? (cp & 3) : 2;
      const int go = (((cp >> 2) & 1) * 3 + m3 + 6 * kq) * 16;
      v4u a;
      for (u32 spin = 0; spin < SPIN_CAP; ++spin) {
        CFENCE();
        a = ld4<SC1>(rws, go, WS_FC + w * 384);
        if (!any(top4(a) == SENT)) break;
      }
      STAMP(10);
      v4f d[6];
#pragma unroll
      for (int i = 0; i < 6; ++i) d[i] = mfma(a, W[i], v4f{0.f, 0.f, 0.f, 0.f});
      const float lo3 = (d[0][0] + d[1][1]) + d[2][2], hi3 = (d[3][0] + d[4][1]) + d[5][2];
      *(float*)(lds + L_R2 + cp * 80 + (w * 2 + (kq & 1)) * 4) = (kq & 1) ? hi3 : lo3;
      BAR();
      if (w == 0) {
        const v4f y = *(const v4f*)(lds + L_R2 + cp * 80 + kq * 16);
        float ffn = (y[0] + y[1]) + (y[2] + y[3]);
        ffn = sw16(ffn, ffn); ffn = sw32(ffn, ffn);
        const __bf16 res = (__bf16)(x1 + rb(gb * rb(ffn)));
        __builtin_amdgcn_raw_buffer_store_b16(__builtin_bit_cast(unsigned short, res), mk(P.out, 1280), l < 16 ? l * 2 : OOB, dch * 2, 0);
        __builtin_amdgcn_raw_buffer_store_b32(tag, rws, (dch == 0 && l == 0) ? WS_E : OOB, 0, SC1);
      }
      STAMP(11);
    }
  }
  asm volatile("" :: "v"(touch[0]), "v"(touch[1]), "v"(touch[2]), "v"(touch[3]));
}

__global__ void __launch_bounds__(512, 2) fused_block(Params P) {
  __shared__ __attribute__((aligned(16))) char lds[L_END];
  STAMP(0);
  const u32 t0 = (u32)__builtin_amdgcn_s_memrealtime();
  const int b = blockIdx.x, l = threadIdx.x & 63, w = __builtin_amdgcn_readfirstlane(threadIdx.x >> 6);
  const int idx = b >> 3;
  const size_t kvbase = ((size_t)(4 * (b & 7) + (idx >> 3)) * SK + (SK - WIN) + (idx & 7) * 512 + w * 64) * (DH * 2);
  const rsrc_t rk = mk((const char*)P.kc + kvbase, 64 * 384);
  const int kvo = (l >> 3) * 384 + (l & 7) * 16;
  u32 cpf = 0;
  {
    u64 pc; u32 len;
    asm volatile("s_getpc_b64 %0\n.Lcode_begin:\n\ts_mov_b32 %1, .Lcode_end-.Lcode_begin" : "=s"(pc), "=s"(len));
    cpf = ld1<0>(mk((const void*)pc, len), l < 2 ? (idx * 16 + w * 2 + l) * 128 : OOB, 0);
    SB();
  }
  v4u K[24];
  const bool isE = (u32)(b - 176) < 20u;
  {
    const v4u e = {SENT, SENT, SENT, SENT};
    st4(mk(P.ws, isE ? WS_END : 0), l < 48 ? l * 16 : OOB, WS_OA + ((b - 176) * 8 + w) * 768, e);
    const rsrc_t r0 = mk(isE ? (const char*)P.wmod + w * 80 * 7680 + (b - 176) * 128 : (const char*)P.kc + kvbase, isE ? 79 * 7680 + 128 : 64 * 384);
    const int vo = (l >> 3) * (isE ? 76800 : 384) + (l & 7) * 16;
#pragma unroll
    for (int i = 0; i < KFIRST; ++i) K[i] = ld4<NT>(r0, vo, isE ? i * 7680 : (i % 3) * 128 + (i / 3) * 3072);
  }
  const u32 ecall = ld1<SC1>(mk(P.ws, 4), 0, WS_E);
  SB();
  STAMP(15);
  if (isE) run<RE>(P, lds, rk, kvbase, kvo, K, cpf, ecall, t0, b, w, l TSPASS);
  else if (idx >= 22) run<RM>(P, lds, rk, kvbase, kvo, K, cpf, ecall, t0, b, w, l TSPASS);
  else if (idx < 12) run<RQ>(P, lds, rk, kvbase, kvo, K, cpf, ecall, t0, b, w, l TSPASS);
  else run<RO>(P, lds, rk, kvbase, kvo, K, cpf, ecall, t0, b, w, l TSPASS);
}
__global__ void code_end_marker() { asm volatile(".Lcode_end:"); }

extern "C" void kernel_launch(void* const* d_in, const int* in_sizes, int n_in, void* d_out, int out_size, void* d_ws,
                              size_t ws_size, hipStream_t stream) {
  Params P;
  P.x = d_in[0]; P.kc = d_in[1]; P.vc = d_in[2]; P.wq = d_in[3]; P.wo = d_in[4];
  P.wg = d_in[6]; P.wu = d_in[7]; P.wd = d_in[8]; P.c = d_in[9]; P.wmod = d_in[10];
  P.out = d_out; P.ws = d_ws;
  fused_block<<<256, 512, 0, stream>>>(P);
}
```
